```python
import torch

def _softmax(x, axis=-1):
    return torch.softmax(x, dim=axis)

def _gelu(x):
    return torch.nn.functional.gelu(x, approximate='tanh')

def _silu(x):
    return torch.nn.functional.silu(x)

def _sigmoid(x):
    return torch.sigmoid(x)

def _layernorm(x, axis=-1, eps=1e-05):
    m = x.mean(dim=axis, keepdim=True)
    v = x.var(dim=axis, keepdim=True, unbiased=False)
    return (x - m) / torch.sqrt(v + eps)

def _scatter_add(tgt, idx, src, axis=0):
    return tgt.clone().index_add_(axis, idx, src)

def _segment_sum(src, seg, n):
    out = torch.zeros((n,) + src.shape[1:], dtype=src.dtype)
    return out.index_add_(0, seg, src)

def _pad(x, pw):
    flat = tuple((v for pair in reversed(pw) for v in pair))
    return torch.nn.functional.pad(x, flat)
kernel_name = 'concat_heads__linear_kernel_hedgehog__memory_aug_recurrent_mem_tr__prompt'

def setup_inputs():

    _w = lambda a: (lambda t: t.to(torch.bfloat16) if t.is_floating_point() else t)(torch.as_tensor(a))

    def _A_setup():
        B, S, D, H = (1, 2048, 448, 16)
        Dh = D // H
        return {'x': _w((lambda *s: torch.randn(*s, dtype=torch.bfloat16) * 0.4)(B, S, D)), 'wqkv': _w((lambda *s: torch.randn(*s, dtype=torch.bfloat16) * 0.4)(D, 3 * D)), 'w_phi': _w((lambda *s: torch.randn(*s, dtype=torch.bfloat16) * 0.4)(H, Dh, Dh) * Dh ** (-0.5)), 'wo': _w((lambda *s: torch.randn(*s, dtype=torch.bfloat16) * 0.05)(D, D))}
    _A_in = _A_setup()

    def _B_setup():
        B, S, Dm = (1, 2048, 448)
        H, Dh = (16, 256)
        M, seg = (8, 32)
        seg = min(seg, S)
        S = S // seg * seg
        return {'x': _w((lambda *s: torch.randn(*s, dtype=torch.bfloat16) * 0.05)(B, S, Dm)), 'mem0': _w((lambda *s: torch.randn(*s, dtype=torch.bfloat16) * 0.8)(B, M, Dm)), 'Wq': _w((lambda *s: torch.randn(*s, dtype=torch.bfloat16) * 1.6)(Dm, H, Dh) * Dm ** (-0.5)), 'Wk': _w((lambda *s: torch.randn(*s, dtype=torch.bfloat16) * 1.6)(Dm, H, Dh) * Dm ** (-0.5)), 'Wv': _w((lambda *s: torch.randn(*s, dtype=torch.bfloat16) * 0.8)(Dm, H, Dh) * Dm ** (-0.5)), 'Wo': _w((lambda *s: torch.randn(*s, dtype=torch.bfloat16) * 0.8)(H, Dh, Dm) * (H * Dh) ** (-0.5)), 'seg_len': seg}
    _B_in = _B_setup()
    return {'A_x': _A_in['x'], 'A_wqkv': _A_in['wqkv'], 'A_w_phi': _A_in['w_phi'], 'A_wo': _A_in['wo'], 'B_x': _B_in['x'], 'B_mem0': _B_in['mem0'], 'B_Wq': _B_in['Wq'], 'B_Wk': _B_in['Wk'], 'B_Wv': _B_in['Wv'], 'B_Wo': _B_in['Wo'], 'B_seg_len': _B_in['seg_len']}

def reference(A_x, A_wqkv, A_w_phi, A_wo, B_x, B_mem0, B_Wq, B_Wk, B_Wv, B_Wo, B_seg_len):

    def _A_ref(x, wqkv, w_phi, wo):
        B, S, D = x.shape
        H = 16
        Dh = D // H
        qkv = (lambda x, *d: x.permute(*d))((x @ wqkv).reshape(B, S, 3, H, Dh), 2, 0, 3, 1, 4)
        q, k, v = (qkv[0], qkv[1], qkv[2])
        pq = torch.einsum('bhsd,hdf->bhsf', q, w_phi)
        pk = torch.einsum('bhsd,hdf->bhsf', k, w_phi)
        phi_q = torch.concatenate([_softmax(pq, axis=-1), _softmax(-pq, axis=-1)], axis=-1)
        phi_k = torch.concatenate([_softmax(pk, axis=-1), _softmax(-pk, axis=-1)], axis=-1)
        kv = torch.einsum('bhsf,bhsd->bhfd', phi_k, v)
        z = torch.einsum('bhsf->bhf', phi_k)
        num = torch.einsum('bhqf,bhfd->bhqd', phi_q, kv)
        den = torch.einsum('bhqf,bhf->bhq', phi_q, z)[..., None] + 1e-06
        o = num / den
        return (lambda x, *d: x.permute(*d))(o, 0, 2, 1, 3).reshape(B, S, D) @ wo

    def _B_ref(x, mem0, Wq, Wk, Wv, Wo, seg_len):
        B, S, Dm = x.shape
        H, Dh = (Wq.shape[1], Wq.shape[2])
        M = mem0.shape[1]
        scale = Dh ** (-0.5)
        n_seg = S // seg_len
        L = M + seg_len
        idx = torch.arange(L)
        mask = (idx[:, None] < M) | (idx[:, None] >= idx[None, :])
        mem = mem0
        outs = []
        for s in range(n_seg):
            xs = x[:, s * seg_len:(s + 1) * seg_len, :].clone()
            h = torch.concatenate([mem, xs], axis=1)
            q = torch.einsum('btd,dhe->bhte', h, Wq)
            k = torch.einsum('btd,dhe->bhte', h, Wk)
            v = torch.einsum('btd,dhe->bhte', h, Wv)
            scores = torch.einsum('bhqe,bhke->bhqk', q, k) * scale
            scores = torch.where(mask[None, None], scores, -1000000000.0)
            p = _softmax(scores, axis=-1)
            o = torch.einsum('bhqk,bhke->bhqe', p, v)
            o = torch.einsum('bhte,hed->btd', o, Wo)
            mem = o[:, :M, :].clone()
            outs.append(o[:, M:, :])
        return torch.concatenate(outs, axis=1)
    a = _A_ref(A_x, A_wqkv, A_w_phi, A_wo)
    b = _B_ref(B_x, B_mem0, B_Wq, B_Wk, B_Wv, B_Wo, B_seg_len)
    return torch.concatenate([a, b], axis=-1)
```

```cpp
#include <hip/hip_runtime.h>
#include <hip/hip_bf16.h>

typedef unsigned short u16;
typedef __bf16 bf16x8 __attribute__((ext_vector_type(8)));
typedef __bf16 bf16x2 __attribute__((ext_vector_type(2)));
typedef short s16x4 __attribute__((ext_vector_type(4)));
typedef float f32x4 __attribute__((ext_vector_type(4)));
typedef unsigned v4u __attribute__((ext_vector_type(4)));
typedef unsigned v2u __attribute__((ext_vector_type(2)));
#define DEV __device__ __forceinline__
#define LAS __attribute__((address_space(3)))
#define GAS __attribute__((address_space(1)))

constexpr int DM = 448, NH = 16, EH = 256, SEQ = 2048, NSEG = 64, KS = 14, CT = 28;
constexpr size_t MB = 1u << 20;
constexpr size_t OFF_XCH = 0;
constexpr size_t XCH_BYTES = 6 * MB;
constexpr size_t OFF_CTRL = 6 * MB;
constexpr size_t OFF_AFR = 8 * MB;
constexpr size_t OFF_ATFR = 16 * MB;
constexpr size_t OFF_BTFR = 24 * MB;
constexpr size_t OFF_XFR = 32 * MB;
constexpr size_t OFF_MFR = 34 * MB;
constexpr size_t OFF_XAFR = 36 * MB;
constexpr size_t OFF_GT = 40 * MB;
constexpr size_t OFF_UT = 72 * MB;
constexpr size_t OFF_RT = 104 * MB;
constexpr size_t OFF_VT = 112 * MB;
constexpr size_t OFF_WQF = 120 * MB;
constexpr size_t OFF_WKF = 121 * MB;
constexpr size_t OFF_WVF = 122 * MB;
constexpr size_t OFF_WOF = 123 * MB;
constexpr size_t OFF_KVP = 124 * MB;
constexpr size_t SEGB = (size_t)NH * CT * 1024;
constexpr int NPAIR = 384;
constexpr unsigned OOB = 0xFFFFFF00u;
constexpr unsigned SPIN_CAP = 1u << 21;
constexpr int SPIN_BUDGET = 1 << 22;
constexpr int CT_VOTE = 0;
constexpr int CT_TICK = 4096;
constexpr int CT_ABORT = 8192;

DEV unsigned char* ws_base(void* ws) { const size_t a = (size_t)ws; return (unsigned char*)ws + (((a + (2 * MB - 1)) & ~(size_t)(2 * MB - 1)) - a); }
DEV unsigned pk2(float a, float b) { bf16x2 v = {(__bf16)a, (__bf16)b}; return __builtin_bit_cast(unsigned, v); }
DEV float bf2f(u16 b) { return __builtin_bit_cast(float, (unsigned)b << 16); }
DEV f32x4 mfma32(v4u a, v4u b, f32x4 c) { return __builtin_amdgcn_mfma_f32_16x16x32_bf16(__builtin_bit_cast(bf16x8, a), __builtin_bit_cast(bf16x8, b), c, 0, 0, 0); }
DEV f32x4 mfma16(v2u a, v2u b, f32x4 c) { return __builtin_amdgcn_mfma_f32_16x16x16bf16_1k(__builtin_bit_cast(s16x4, a), __builtin_bit_cast(s16x4, b), c, 0, 0, 0); }
DEV v4u pack8(f32x4 a, f32x4 b) { v4u r; r.x = pk2(a[0], a[1]); r.y = pk2(a[2], a[3]); r.z = pk2(b[0], b[1]); r.w = pk2(b[2], b[3]); return r; }
DEV int sig(int tau, int rho) { return 8 * (rho >> 2) + 4 * tau + (rho & 3); }
DEV unsigned xcc_id() { return (unsigned)__builtin_amdgcn_s_getreg(20 | (3 << 11)) & 0xfu; }
DEV int rfl(int x) { return __builtin_amdgcn_readfirstlane(x); }
DEV float f_as(unsigned x) { return __builtin_bit_cast(float, x); }
DEV unsigned u_as(float x) { return __builtin_bit_cast(unsigned, x); }
template <int CTRL> DEV float dppf(float x) { return f_as((unsigned)__builtin_amdgcn_update_dpp(0, (int)u_as(x), CTRL, 0xf, 0xf, true)); }
template <int CTRL> DEV unsigned dppu(unsigned x) { return (unsigned)__builtin_amdgcn_update_dpp(0, (int)x, CTRL, 0xf, 0xf, true); }
constexpr int DPP_ROR8 = 0x128, DPP_ROR4 = 0x124, DPP_XOR1 = 0xB1, DPP_XOR2 = 0x4E;
DEV float vmax(float a, float b) { return __builtin_elementwise_maximum(a, b); }
DEV float xg_max(float m) {
  auto a = __builtin_amdgcn_permlane32_swap(u_as(m), u_as(m), false, false);
  m = vmax(f_as(a[0]), f_as(a[1]));
  auto b = __builtin_amdgcn_permlane16_swap(u_as(m), u_as(m), false, false);
  return vmax(f_as(b[0]), f_as(b[1]));
}
DEV float xg_sum(float m) {
  auto a = __builtin_amdgcn_permlane32_swap(u_as(m), u_as(m), false, false);
  m = f_as(a[0]) + f_as(a[1]);
  auto b = __builtin_amdgcn_permlane16_swap(u_as(m), u_as(m), false, false);
  return f_as(b[0]) + f_as(b[1]);
}
DEV v4u ld_sc1(__amdgpu_buffer_rsrc_t r, unsigned off) { return __builtin_amdgcn_raw_buffer_load_b128(r, off, 0, 16); }
DEV void st_sc1(__amdgpu_buffer_rsrc_t r, unsigned off, v4u v) { __builtin_amdgcn_raw_buffer_store_b128(v, r, off, 0, 16); }
DEV bool frag_valid(v4u v) { unsigned m = max(max(v.x, v.y), max(v.z, v.w)); return m != 0xFFFFFFFFu; }
DEV unsigned long long memtime() { unsigned long long t; asm volatile("s_memtime %0\n\ts_waitcnt lgkmcnt(0)" : "=s"(t) :: "memory"); return t; }

#define STAMPV(role, s, wave, k) do {} while (0)
#define STAMPDUMP(role) do {} while (0)
struct In {
  const u16 *Ax, *Awqkv, *Awphi, *Awo, *Bx, *Bmem0, *Wq, *Wk, *Wv, *Wo;
  u16* out; void* ws;
};

DEV void prep_wprod(const In& in, unsigned char* wb, int unit, int lane) {
  const int cb = unit % 7, rb = (unit / 7) % 7, h = (unit / 49) % 16, m = unit / 784;
  const int rho = lane & 15, g = lane >> 4;
  const u16* P = m == 0 ? in.Wk : (m == 1 ? in.Wq : in.Wv);
  const u16* Q = m == 0 ? in.Wq : in.Wk;
  f32x4 acc[4][4];
#pragma unroll
  for (int a = 0; a < 4; ++a)
#pragma unroll
    for (int b = 0; b < 4; ++b) acc[a][b] = (f32x4){0.f, 0.f, 0.f, 0.f};
  int prow[4], qcol[4];
#pragma unroll
  for (int a = 0; a < 4; ++a) {
    prow[a] = rb * 64 + 32 * (a >> 1) + sig(a & 1, rho);
    qcol[a] = (m == 2) ? cb * 64 + 16 * a + rho : cb * 64 + 32 * (a >> 1) + sig(a & 1, rho);
  }
  for (int es = 0; es < 8; ++es) {
    v4u pa[4], qb[4];
    const int e0 = h * EH + es * 32 + g * 8;
#pragma unroll
    for (int a = 0; a < 4; ++a) pa[a] = *(const v4u*)(P + (size_t)prow[a] * 4096 + e0);
    if (m != 2) {
#pragma unroll
      for (int a = 0; a < 4; ++a) qb[a] = *(const v4u*)(Q + (size_t)qcol[a] * 4096 + e0);
    } else {
#pragma unroll
      for (int a = 0; a < 4; ++a) {
        const u16* w = in.Wo + (size_t)e0 * DM + qcol[a];
        unsigned t[8];
#pragma unroll
        for (int j = 0; j < 8; ++j) t[j] = w[j * DM];
        qb[a] = (v4u){t[0] | (t[1] << 16), t[2] | (t[3] << 16), t[4] | (t[5] << 16), t[6] | (t[7] << 16)};
      }
    }
#pragma unroll
    for (int a = 0; a < 4; ++a)
#pragma unroll
      for (int b = 0; b < 4; ++b) acc[a][b] = mfma32(pa[a], qb[b], acc[a][b]);
  }
  const float sc = (m == 2) ? 1.f : 0.0625f * 1.4426950408889634f;
  unsigned char* dst = wb + (m == 0 ? OFF_AFR : (m == 1 ? OFF_ATFR : OFF_BTFR));
#pragma unroll
  for (int b = 0; b < 4; ++b)
#pragma unroll
    for (int ap = 0; ap < 2; ++ap) {
      v4u o = pack8(acc[2 * ap][b] * sc, acc[2 * ap + 1][b] * sc);
      const size_t fi = ((size_t)(h * CT + cb * 4 + b) * KS + rb * 2 + ap);
      *(v4u*)(dst + fi * 1024 + lane * 16) = o;
    }
}

__global__ void __launch_bounds__(256) k_probe(In in) {
  unsigned char* wb = ws_base(in.ws);
  const int tid = threadIdx.x, lane = tid & 63, wave = rfl(tid >> 6);
  const int gw = blockIdx.x * 4 + wave;
  if (gw < NPAIR) {
    if (lane == 0) {
      unsigned* p0 = (unsigned*)(wb + OFF_XCH + (size_t)gw * 16384 + 8064);
      unsigned* p1 = p0 + 2048;
      unsigned t0 = ~0u, t1 = ~0u;
      for (int rep = 0; rep < 3; ++rep) {
        unsigned v, v2, z = 0;
        unsigned long long a = memtime();
        asm volatile("global_atomic_or %0, %1, %2, off sc0\n\ts_waitcnt vmcnt(0)" : "=v"(v) : "v"(p0), "v"(z) : "memory");
        unsigned long long b = memtime();
        asm volatile("global_atomic_or %0, %1, %2, off sc0\n\ts_waitcnt vmcnt(0)" : "=v"(v2) : "v"(p1), "v"(z) : "memory");
        unsigned long long c = memtime();
        t0 = min(t0, (unsigned)(b - a)); t1 = min(t1, (unsigned)(c - b));
      }
      int near1 = t1 < t0;
      int far_iod = xcc_id() >= 4;
      int vote = near1 ^ far_iod;
      unsigned d = t0 > t1 ? t0 - t1 : t1 - t0;
      ((int*)(wb + OFF_CTRL + CT_VOTE))[gw] = d > 100 ? vote : 2;
    }
  }
}

__global__ void __launch_bounds__(256) k_prep(In in) {
  unsigned char* wb = ws_base(in.ws);
  const int tid = threadIdx.x, lane = tid & 63, wave = rfl(tid >> 6);
  const int gw = blockIdx.x * 4 + wave, nw = gridDim.x * 4;
  const int gt = blockIdx.x * 256 + tid, nt = gridDim.x * 256;
  if (gt < 8) *(unsigned*)(wb + OFF_CTRL + CT_TICK + gt * 256) = 0u;
  if (gt == 8) *(unsigned*)(wb + OFF_CTRL + CT_ABORT) = 0u;
  for (size_t o = (size_t)gt * 16; o < XCH_BYTES; o += (size_t)nt * 16) {
    v4u v = {~0u, ~0u, ~0u, ~0u};
    if (o < 16384) {
      const int w = (int)(o & 8191);
      if (w < 7168) {
        const int slot = w >> 7, cg = slot >> 1, rg = slot & 1, ii = (w >> 5) & 3, hf = (w >> 4) & 1;
        v = *(const v4u*)(in.Bmem0 + (rg * 4 + ii) * DM + cg * 16 + hf * 8);
        v.x = min(v.x, 0xFFFFFFFEu); v.y = min(v.y, 0xFFFFFFFEu); v.z = min(v.z, 0xFFFFFFFEu); v.w = min(v.w, 0xFFFFFFFEu);
      }
    }
    *(v4u*)(wb + OFF_XCH + o) = v;
  }
  for (int i = gt; i < 2 * NSEG * 2 * KS * 64; i += nt) {
    const int part = i / (NSEG * 2 * KS * 64), j = i % (NSEG * 2 * KS * 64);
    const int l = j & 63, f = j >> 6, cs = f % KS, tau = (f / KS) & 1, s = f / (2 * KS);
    const int row = s * 32 + sig(tau, l & 15);
    const u16* src = (part ? in.Ax : in.Bx) + (size_t)row * DM + cs * 32 + (l >> 4) * 8;
    *(v4u*)(wb + (part ? OFF_XAFR : OFF_XFR) + (size_t)j * 16) = *(const v4u*)src;
  }
  for (int i = gt; i < 3 * NH * 2 * KS * 64; i += nt) {
    const int which = i / (NH * 2 * KS * 64), j = i % (NH * 2 * KS * 64);
    const int l = j & 63, f = j >> 6, ks = f % KS, tau = (f / KS) & 1, h = f / (2 * KS);
    const int feat = sig(tau, l & 15), k0 = ks * 32 + (l >> 4) * 8;
    float r[8];
#pragma unroll
    for (int jj = 0; jj < 8; ++jj) r[jj] = 0.f;
    if (feat < 28) {
      if (which == 2) {
#pragma unroll
        for (int jj = 0; jj < 8; ++jj) r[jj] = bf2f(in.Awqkv[(size_t)(k0 + jj) * 1344 + 896 + h * 28 + feat]);
      } else {
        for (int d = 0; d < 28; ++d) {
          const float wp = bf2f(in.Awphi[(h * 28 + d) * 28 + feat]) * 1.4426950408889634f;
#pragma unroll
          for (int jj = 0; jj < 8; ++jj) r[jj] += bf2f(in.Awqkv[(size_t)(k0 + jj) * 1344 + which * 448 + h * 28 + d]) * wp;
        }
      }
    }
    v4u o = {pk2(r[0], r[1]), pk2(r[2], r[3]), pk2(r[4], r[5]), pk2(r[6], r[7])};
    *(v4u*)(wb + (which == 0 ? OFF_WQF : (which == 1 ? OFF_WKF : OFF_WVF)) + (size_t)j * 16) = o;
  }
  for (int i = gt; i < NH * CT * 64; i += nt) {
    const int l = i & 63, f = i >> 6, ntile = f % CT, h = f / CT;
    const int n = 32 * (ntile >> 1) + sig(ntile & 1, l & 15), d0 = (l >> 4) * 8;
    unsigned t[8];
#pragma unroll
    for (int jj = 0; jj < 8; ++jj) t[jj] = (d0 + jj < 28) ? in.Awo[(size_t)(h * 28 + d0 + jj) * DM + n] : 0u;
    v4u o = {t[0] | (t[1] << 16), t[2] | (t[3] << 16), t[4] | (t[5] << 16), t[6] | (t[7] << 16)};
    *(v4u*)(wb + OFF_WOF + (size_t)i * 16) = o;
  }
  for (int u = gw; u < 3 * 16 * 49; u += nw) prep_wprod(in, wb, u, lane);
}

template <bool ISG>
DEV void gu_worker(const unsigned char* wfr, const unsigned char* xfr, unsigned char* outp, int T0, int s_begin, int s_end,
                   unsigned char* lds, int tid) {
  const int lane = tid & 63, wave = rfl(tid >> 6), tp = wave & 3, sp = wave >> 2;
  v4u W[2][KS];
#pragma unroll
  for (int t = 0; t < 2; ++t)
#pragma unroll
    for (int k = 0; k < KS; ++k) W[t][k] = *(const v4u*)(wfr + ((size_t)(T0 + 2 * tp + t) * KS + k) * 1024 + lane * 16);
  const int T = T0 + 2 * tp;
  auto stage = [&](int s, int buf) {
#pragma unroll
    for (int i = 0; i < 7; ++i)
      __builtin_amdgcn_global_load_lds((const unsigned*)(xfr + (size_t)s * 28672 + i * 8192 + tid * 16),
                                       (__attribute__((address_space(3))) unsigned*)(lds + buf * 57344 + i * 8192 + wave * 1024), 16, 0, 0);
  };
  stage(s_begin, 0);
  int buf = 0;
  asm volatile("s_waitcnt vmcnt(0)" ::: "memory");
  for (int s = s_begin; s < s_end; s += 2) {
    asm volatile("s_waitcnt vmcnt(2) lgkmcnt(0)\n\ts_barrier" ::: "memory");
    stage(s + 2 < s_end ? s + 2 : s, buf ^ 1);
    const unsigned char* xb = lds + buf * 57344 + sp * 28672 + lane * 16;
    f32x4 acc[2][2];
#pragma unroll
    for (int a = 0; a < 2; ++a)
#pragma unroll
      for (int b = 0; b < 2; ++b) acc[a][b] = (f32x4){0.f, 0.f, 0.f, 0.f};
#pragma unroll
    for (int k = 0; k < KS; ++k) {
      v4u x0 = *(const v4u*)(xb + k * 1024), x1 = *(const v4u*)(xb + (KS + k) * 1024);
      if (ISG) {
        acc[0][0] = mfma32(W[0][k], x0, acc[0][0]); acc[1][0] = mfma32(W[1][k], x0, acc[1][0]);
        acc[0][1] = mfma32(W[0][k], x1, acc[0][1]); acc[1][1] = mfma32(W[1][k], x1, acc[1][1]);
      } else {
        acc[0][0] = mfma32(x0, W[0][k], acc[0][0]); acc[1][0] = mfma32(x0, W[1][k], acc[1][0]);
        acc[0][1] = mfma32(x1, W[0][k], acc[0][1]); acc[1][1] = mfma32(x1, W[1][k], acc[1][1]);
      }
    }
    unsigned char* ob = outp + (size_t)(s + sp) * SEGB + lane * 16;
    if (ISG) {
      const int h = T / CT, ks = (T % CT) >> 1;
      *(v4u*)(ob + ((size_t)(h * 2 + 0) * KS + ks) * 1024) = pack8(acc[0][0], acc[1][0]);
      *(v4u*)(ob + ((size_t)(h * 2 + 1) * KS + ks) * 1024) = pack8(acc[0][1], acc[1][1]);
    } else {
      *(v4u*)(ob + (size_t)T * 1024) = pack8(acc[0][0], acc[0][1]);
      *(v4u*)(ob + (size_t)(T + 1) * 1024) = pack8(acc[1][0], acc[1][1]);
    }
    buf ^= 1;
  }
}

__global__ void __launch_bounds__(512, 2) k_gu(In in, int mem_pass) {
  extern __shared__ __attribute__((aligned(16))) unsigned char lds[];
  unsigned char* wb = ws_base(in.ws);
  const int w = blockIdx.x % 112, half = blockIdx.x / 112;
  const int nseg = mem_pass ? 16 : NSEG;
  const int s0 = half * (nseg / 2), s1 = s0 + nseg / 2;
  const unsigned char* xfr = wb + (mem_pass ? OFF_MFR : OFF_XFR);
  if (w < 56) gu_worker<true>(wb + OFF_AFR, xfr, wb + (mem_pass ? OFF_RT : OFF_GT), 8 * w, s0, s1, lds, threadIdx.x);
  else gu_worker<false>(wb + OFF_BTFR, xfr, wb + (mem_pass ? OFF_VT : OFF_UT), 8 * (w - 56), s0, s1, lds, threadIdx.x);
}

DEV unsigned read_parity(const unsigned char* wb, int lane) {
  const int* v = (const int*)(wb + OFF_CTRL + CT_VOTE);
  unsigned par = 0;
#pragma unroll
  for (int p = 0; p < 3; ++p) {
    int a = v[p * 128 + lane], b = v[p * 128 + 64 + lane];
    int n1 = __popcll(__ballot(a == 1)) + __popcll(__ballot(b == 1));
    int n0 = __popcll(__ballot(a == 0)) + __popcll(__ballot(b == 0));
    par |= (n1 > n0 ? 1u : 0u) << p;
  }
  return par;
}
DEV unsigned pair_off(unsigned par, int p) { return (unsigned)p * 16384u + (((par >> (p >> 7)) & 1u) << 13); }
DEV unsigned memx_off(unsigned par, int s) { return pair_off(par, s); }
DEV unsigned scx_off(unsigned par, int s, int rg, int h) { return pair_off(par, 64 + 4 * s + 2 * rg + (h >> 3)) + (h & 7) * 1024; }

DEV bool any_invalid(v4u v) { unsigned m = max(max(v.x, v.y), max(v.z, v.w)); return __any(m == 0xFFFFFFFFu); }
DEV v4u poll1(__amdgpu_buffer_rsrc_t rx, unsigned off, int& budget) {
  v4u t;
#pragma nounroll
  do { asm volatile("" ::: "memory"); t = ld_sc1(rx, off); --budget; } while (any_invalid(t) && budget > 0);
  return t;
}
#define GATE(c) do { if (__builtin_expect(!((ready >> (c)) & 1u), 0)) { \
    _Pragma("nounroll") do { ready = (unsigned)__ballot(flags[lane & 7] == epoch); --budget; } while (!((ready >> (c)) & 1u) && budget > 0); } } while (0)
#define BARRIER() asm volatile("s_waitcnt lgkmcnt(0)\n\ts_barrier" ::: "memory")

template <bool HASG>
DEV void role_S(unsigned char* wb, int h, int q, unsigned char* lds, int tid) {
  const int lane = tid & 63, wave = rfl(tid >> 6);
  float* red = (float*)lds;
  volatile LAS unsigned* flags = (volatile LAS unsigned*)(lds + 3072);
  unsigned char* mimg = lds + 4096;
  unsigned char* gring = lds + 12288;
  if (tid < 16) flags[tid] = 0u;
  const unsigned par = read_parity(wb, lane);
  __amdgpu_buffer_rsrc_t rx = __builtin_amdgcn_make_buffer_rsrc(wb + OFF_XCH, 0, (int)XCH_BYTES, 0x00020000);
  int budget = SPIN_BUDGET;
  if (wave == 7) {
    if (!HASG) { BARRIER(); return; }
    const unsigned char* gsrc = wb + OFF_GT + ((size_t)(h * 2 + q) * KS) * 1024;
    auto issue = [&](int s) {
#pragma unroll
      for (int k = 0; k < KS; ++k)
        __builtin_amdgcn_global_load_lds((const unsigned*)(gsrc + (size_t)s * SEGB + k * 1024 + lane * 16),
                                         (LAS unsigned*)(gring + (s & 1) * 14336 + k * 1024), 16, 0, 0);
    };
    issue(0);
    asm volatile("s_waitcnt vmcnt(0) lgkmcnt(0)\n\ts_barrier" ::: "memory");
    issue(1);
    for (int s = 0; s < NSEG - 1; ++s) {
      asm volatile("s_barrier" ::: "memory");
      asm volatile("s_waitcnt vmcnt(0)\n\ts_barrier" ::: "memory");
      if (s + 2 < NSEG - 1) issue(s + 2);
    }
    return;
  }
  if (wave >= 4) {
    BARRIER();
    for (int s = 0; s < NSEG - 1; ++s) {
      const v4u t = poll1(rx, memx_off(par, s) + tid * 16, budget);
      *(v4u*)(mimg + tid * 16) = t;
      STAMPV(h * 4 + q, s, wave, 0);
      BARRIER();
      BARRIER();
    }
    STAMPDUMP(h * 4 + q);
    return;
  }
  const int cp0 = q == 0 ? 0 : (q == 1 ? 3 : (q == 2 ? 6 : 10));
  const int i = lane & 15, g = lane >> 4, i8 = i & 7;
  const unsigned char* fbase = mimg + (g >> 1) * 256 + (i8 >> 2) * 128 + (i8 & 3) * 32 + (g & 1) * 16;
  const bool act = i < 8;
  if (HASG && wave == 3) {
    BARRIER();
    const unsigned pl = (unsigned)(512 + q * 256 + (i & 3) * 64 + g * 16);
    for (int s = 0; s < NSEG - 1; ++s) {
      const unsigned epoch = (unsigned)(s + 1);
      v4u G[KS];
#pragma unroll
      for (int k = 0; k < KS; ++k) G[k] = *(const v4u*)(gring + (s & 1) * 14336 + k * 1024 + lane * 16);
      const unsigned pubo = act ? scx_off(par, s, i >> 2, h) + pl : OOB;
      const v4u t = poll1(rx, memx_off(par, s) + tid * 16, budget);
      *(v4u*)(mimg + tid * 16) = t;
      BARRIER();
      f32x4 acc = {0.f, 0.f, 0.f, 0.f};
      v4u F[KS];
#pragma unroll
      for (int k = 0; k < KS; ++k) F[k] = *(const v4u*)(fbase + k * 512);
      __builtin_amdgcn_sched_barrier(0);
#pragma unroll
      for (int k = 0; k < KS; ++k) acc = mfma32(G[k], F[k], acc);
      v4u o = {min(u_as(acc[0]), 0xFFFFFFFEu), min(u_as(acc[1]), 0xFFFFFFFEu), min(u_as(acc[2]), 0xFFFFFFFEu), min(u_as(acc[3]), 0xFFFFFFFEu)};
      st_sc1(rx, pubo, o);
      STAMPV(h * 4 + q, s, wave, 2);
      BARRIER();
    }
    STAMPDUMP(h * 4 + q);
    return;
  }
  constexpr int NW = HASG ? 3 : 4;
  const int cp = cp0 + wave;
  v4u W[2][KS];
#pragma unroll
  for (int t = 0; t < 2; ++t)
#pragma unroll
    for (int k = 0; k < KS; ++k) W[t][k] = *(const v4u*)(wb + OFF_ATFR + ((size_t)(h * CT + 2 * cp + t) * KS + k) * 1024 + lane * 16);
  const int pii = (lane >> 1) & 7;
  const unsigned pub_l = (unsigned)(q * 128 + (pii & 3) * 32 + (lane & 1) * 16);
  const int wofs = (act && g < 2) ? wave * 64 + i * 8 + 4 * g : 256 + lane * 4;
  BARRIER();
  for (int s = 0; s < NSEG - 1; ++s) {
    const unsigned epoch = (unsigned)(s + 1);
    const unsigned pubo = lane < 16 ? scx_off(par, s, pii >> 2, h) + pub_l : OOB;
    const v4u t = poll1(rx, memx_off(par, s) + tid * 16, budget);
    *(v4u*)(mimg + tid * 16) = t;
    BARRIER();
    f32x4 a0 = {0.f, 0.f, 0.f, 0.f}, a1 = {0.f, 0.f, 0.f, 0.f};
    v4u F[KS], Fc;
#pragma unroll
    for (int k = 0; k < KS; ++k) F[k] = *(const v4u*)(fbase + k * 512);
    Fc = *(const v4u*)(fbase + cp * 512);
    __builtin_amdgcn_sched_barrier(0);
#pragma unroll
    for (int k = 0; k < KS; ++k) { a0 = mfma32(W[0][k], F[k], a0); a1 = mfma32(W[1][k], F[k], a1); }
    STAMPV(h * 4 + q, s, wave, 0);
    f32x4 z = {0.f, 0.f, 0.f, 0.f};
    const f32x4 acc2 = mfma32(Fc, pack8(a0, a1), z);
    *(f32x4*)(red + wofs) = acc2;
    STAMPV(h * 4 + q, s, wave, 1);
    BARRIER();
    if (wave == 0) {
      const int rl = lane & 15;
      f32x4 r = *(const f32x4*)(red + rl * 4) + *(const f32x4*)(red + 64 + rl * 4);
      if (NW == 4) r = r + (*(const f32x4*)(red + 128 + rl * 4) + *(const f32x4*)(red + 192 + rl * 4));
      else r = r + *(const f32x4*)(red + 128 + rl * 4);
      v4u o = {min(u_as(r[0]), 0xFFFFFFFEu), min(u_as(r[1]), 0xFFFFFFFEu), min(u_as(r[2]), 0xFFFFFFFEu), min(u_as(r[3]), 0xFFFFFFFEu)};
      st_sc1(rx, pubo, o);
    }
    STAMPV(h * 4 + q, s, wave, 2);
  }
  if (budget <= 0) *(GAS unsigned*)(wb + OFF_CTRL + CT_ABORT) = 1u;
  STAMPDUMP(h * 4 + q);
}

DEV void role_O(unsigned char* wb, int rg, int cg, unsigned char* lds, int tid) {
  const int lane = tid & 63, wave = rfl(tid >> 6);
  const int rho = lane & 15, g = lane >> 4;
  const unsigned par = read_parity(wb, lane);
  __amdgpu_buffer_rsrc_t rx = __builtin_amdgcn_make_buffer_rsrc(wb + OFF_XCH, 0, (int)XCH_BYTES, 0x00020000);
  int budget = SPIN_BUDGET;
  v4u W[2][KS], U[2], Un[2];
#pragma unroll
  for (int hh = 0; hh < 2; ++hh)
#pragma unroll
    for (int k = 0; k < KS; ++k) W[hh][k] = *(const v4u*)(wb + OFF_BTFR + ((size_t)((2 * wave + hh) * CT + cg) * KS + k) * 1024 + lane * 16);
  const unsigned char* ubase = wb + OFF_UT + ((size_t)(2 * wave) * CT + cg) * 1024 + lane * 16;
  U[0] = *(const v4u*)(ubase); U[1] = *(const v4u*)(ubase + CT * 1024);
  unsigned char* mimg = lds;
  float* red = (float*)(lds + 8192);
  const int jrow = (rho >> 2) + 4 * (rho & 1);
  const unsigned aoff = (unsigned)((g >> 1) * 256 + (jrow >> 2) * 128 + (jrow & 3) * 32 + (g & 1) * 16);
  const int u = rho >> 3, hh_l = (rho >> 2) & 1, ii = rho & 3;
  const int hl = 2 * wave + hh_l;
  const unsigned off_mm = (unsigned)(g * 128 + ii * 32 + u * 16);
  const unsigned off_mx = (unsigned)(512 + u * 256 + ii * 64 + g * 16);
  const int wpos = (g + 4 * ii) * 20 + hl + (u ? 1280 : 0);
  const int rpos = (((lane & 15) >> 2) + 4 * (lane >> 4) + 16 * (lane & 3)) * 20;
  const unsigned mlane = wave < 7 ? (unsigned)tid * 16u : OOB;
  const unsigned publ = (lane & 1) ? OOB : (unsigned)((cg * 2 + rg) * 128 + lane * 2);

  for (int s = 0; s < NSEG - 1; ++s) {
    {
      const v4u t = poll1(rx, wave < 7 ? memx_off(par, s) + mlane : OOB, budget);
      *(v4u*)(mimg + tid * 16) = t;
    }
    BARRIER();
    STAMPV(64 + cg * 2 + rg, s, wave, 0);
    { const int sn = min(s + 1, NSEG - 2); Un[0] = *(const v4u*)(ubase + (size_t)sn * SEGB); Un[1] = *(const v4u*)(ubase + (size_t)sn * SEGB + CT * 1024); }
    __builtin_amdgcn_sched_barrier(0);
    f32x4 av0 = {0.f, 0.f, 0.f, 0.f}, av1 = {0.f, 0.f, 0.f, 0.f};
    v4u af[KS];
#pragma unroll
    for (int k = 0; k < KS; ++k) af[k] = *(const v4u*)(mimg + k * 512 + aoff);
    __builtin_amdgcn_sched_barrier(0);
#pragma unroll
    for (int k = 0; k < KS; ++k) { av0 = mfma32(af[k], W[0][k], av0); av1 = mfma32(af[k], W[1][k], av1); }
    const v2u vf0 = {pk2(av0[0], av0[1]), 0u}, vf1 = {pk2(av1[0], av1[1]), 0u};
    asm volatile("" :: "v"(vf0), "v"(vf1));
    STAMPV(64 + cg * 2 + rg, s, wave, 1);
    const unsigned sb = scx_off(par, s, rg, hl);
    const unsigned pubo = (lane & 1) ? OOB : memx_off(par, s + 1) + publ;
    v4u tm, tx;
    {
      const unsigned om = sb + off_mm, ox = sb + off_mx;
      bool bad;
#pragma nounroll
      do {
        asm volatile("" ::: "memory");
        tm = ld_sc1(rx, om); tx = ld_sc1(rx, ox);
        --budget;
        const unsigned m1 = max(max(tm.x, tm.y), max(tm.z, tm.w)), m2 = max(max(tx.x, tx.y), max(tx.z, tx.w));
        bad = __any(max(m1, m2) == 0xFFFFFFFFu);
      } while (bad && budget > 0);
    }
    STAMPV(64 + cg * 2 + rg, s, wave, 2);
    auto p0 = __builtin_amdgcn_permlane32_swap(tm.x, tm.z, false, false);
    auto p1 = __builtin_amdgcn_permlane32_swap(tm.y, tm.w, false, false);
    float a0 = f_as(p0[0]) + f_as(p0[1]), a1 = f_as(p1[0]) + f_as(p1[1]);
    auto p2 = __builtin_amdgcn_permlane16_swap(u_as(a0), u_as(a1), false, false);
    const float sm = f_as(p2[0]) + f_as(p2[1]);
    const float x0 = f_as(tx.x), x1 = f_as(tx.y), x2 = f_as(tx.z), x3 = f_as(tx.w);
    float m = vmax(vmax(vmax(x0, x1), x2), vmax(x3, sm));
    m = vmax(m, dppf<DPP_ROR8>(m));
    m = xg_max(m);
    const float e0 = __builtin_amdgcn_exp2f(x0 - m), e1 = __builtin_amdgcn_exp2f(x1 - m), e2 = __builtin_amdgcn_exp2f(x2 - m), e3 = __builtin_amdgcn_exp2f(x3 - m);
    const float em = __builtin_amdgcn_exp2f(sm - m);
    const unsigned q01 = pk2(e0, e1), q23 = pk2(e2, e3);
    const v4u bx = {q01, q23, dppu<DPP_ROR8>(q01), dppu<DPP_ROR8>(q23)};
    const v2u bm = {pk2(em, dppf<DPP_ROR8>(em)), 0u};
    f32x4 z = {0.f, 0.f, 0.f, 0.f};
    f32x4 o0 = mfma32(U[0], bx, z), o1 = mfma32(U[1], bx, z);
    o0 = mfma16(vf0, bm, o0); o1 = mfma16(vf1, bm, o1);
    float l = ((e0 + e1) + (e2 + e3)) + em;
    l += dppf<DPP_ROR8>(l);
    l = xg_sum(l);
    const float il = __builtin_amdgcn_rcpf(l);
#pragma unroll
    for (int r = 0; r < 4; ++r) red[wpos + 320 * r] = (hh_l ? o1[r] : o0[r]) * il;
    BARRIER();
    if (wave == 0) {
      f32x4 a = *(const f32x4*)(red + rpos), b = *(const f32x4*)(red + rpos + 4), c = *(const f32x4*)(red + rpos + 8), d = *(const f32x4*)(red + rpos + 12);
      f32x4 r4 = (a + b) + (c + d);
      float v = (r4[0] + r4[1]) + (r4[2] + r4[3]);
      unsigned w = min(pk2(v, dppf<DPP_XOR1>(v)), 0xFFFFFFFEu);
      __builtin_amdgcn_raw_buffer_store_b32(w, rx, pubo, 0, 16);
    }
    STAMPV(64 + cg * 2 + rg, s, wave, 3);
    U[0] = Un[0]; U[1] = Un[1];
  }
  if (budget <= 0) *(GAS unsigned*)(wb + OFF_CTRL + CT_ABORT) = 1u;
  STAMPDUMP(64 + cg * 2 + rg);
}

DEV int get_slot(unsigned char* wb, unsigned char* lds, int tid) {
  int* sl = (int*)lds;
  if (tid == 0) {
    unsigned* tick = (unsigned*)(wb + OFF_CTRL + CT_TICK);
    const unsigned x = xcc_id() & 7u;
    const unsigned r = __hip_atomic_fetch_add(tick + x * 64, 1u, __ATOMIC_RELAXED, __HIP_MEMORY_SCOPE_AGENT);
    int slot;
    if (r < 32) slot = (int)(x * 32 + r);
    else {
      unsigned c[8]; unsigned spins = 0;
      for (;;) {
        unsigned tot = 0;
        for (int k = 0; k < 8; ++k) { c[k] = __hip_atomic_load(tick + k * 64, __ATOMIC_RELAXED, __HIP_MEMORY_SCOPE_AGENT); tot += c[k]; }
        if (tot >= 256u || ++spins > SPIN_CAP) break;
      }
      unsigned idx = r - 32;
      for (unsigned k = 0; k < x; ++k) idx += c[k] > 32 ? c[k] - 32 : 0;
      slot = -1;
      for (int k = 0; k < 8; ++k) {
        const unsigned fr = c[k] < 32 ? 32 - c[k] : 0;
        if (slot < 0) { if (idx < fr) slot = (int)(k * 32 + c[k] + idx); else idx -= fr; }
      }
    }
    sl[0] = slot;
  }
  __syncthreads();
  const int slot = rfl(sl[0]);
  __syncthreads();
  return slot;
}

__global__ void __launch_bounds__(512, 2) k_rec(In in) {
  extern __shared__ __attribute__((aligned(16))) unsigned char lds[];
  unsigned char* wb = ws_base(in.ws);
  const int tid = threadIdx.x;
  const int slot = get_slot(wb, lds, tid);
  if (slot < 0 || slot >= 128) return;
  const int idx = (slot & 31) * 4 + (slot >> 5);
  if (idx < 64) {
    const int h = idx >> 2, q = idx & 3;
    if (q < 2) role_S<true>(wb, h, q, lds, tid); else role_S<false>(wb, h, q, lds, tid);
  } else if (idx < 120) {
    const int o = idx - 64;
    role_O(wb, o & 1, o >> 1, lds, tid);
  }
}

__global__ void __launch_bounds__(256) k_mfr(In in) {
  unsigned char* wb = ws_base(in.ws);
  const int lane = threadIdx.x & 63;
  const unsigned par = read_parity(wb, lane);
  const int j = blockIdx.x * 256 + threadIdx.x;
  const int l = j & 63, f = j >> 6, ks = f % KS, tau = (f / KS) & 1, grp = f / (2 * KS);
  const int row = sig(tau, l & 15), s = grp * 4 + (row >> 3), i = row & 7, g = l >> 4;
  const unsigned off = memx_off(par, s) + ks * 512 + (g >> 1) * 256 + (i >> 2) * 128 + (i & 3) * 32 + (g & 1) * 16;
  *(v4u*)(wb + OFF_MFR + (size_t)j * 16) = *(const v4u*)(wb + OFF_XCH + off);
}

__global__ void __launch_bounds__(512, 2) k_xrow(In in) {
  extern __shared__ __attribute__((aligned(16))) unsigned char lds[];
  unsigned char* wb = ws_base(in.ws);
  const int tid = threadIdx.x, lane = tid & 63, wave = rfl(tid >> 6);
  const int s = blockIdx.x, grp = s >> 2, a = s & 3;
  const int rho = lane & 15, g = lane >> 4;
  unsigned char* xs = lds;
  unsigned char* ps = lds + 28672;
  for (int i = tid; i < 1792; i += 512) *(v4u*)(xs + i * 16) = *(const v4u*)(wb + OFF_XFR + (size_t)s * 28672 + i * 16);
  __syncthreads();
  for (int hh = 0; hh < 2; ++hh) {
    const int h = 2 * wave + hh;
    const unsigned char* gp = wb + OFF_GT + (size_t)s * SEGB + (size_t)h * 2 * KS * 1024 + lane * 16;
    const unsigned char* rp = wb + OFF_RT + (size_t)grp * SEGB + (size_t)h * 2 * KS * 1024 + lane * 16;
    f32x4 sx[2][2], sm[2][2];
#pragma unroll
    for (int p = 0; p < 2; ++p)
#pragma unroll
      for (int t = 0; t < 2; ++t) { sx[p][t] = (f32x4){0.f, 0.f, 0.f, 0.f}; sm[p][t] = (f32x4){0.f, 0.f, 0.f, 0.f}; }
#pragma unroll 2
    for (int k = 0; k < KS; ++k) {
      v4u g0 = *(const v4u*)(gp + k * 1024), g1 = *(const v4u*)(gp + (KS + k) * 1024);
      v4u r0 = *(const v4u*)(rp + k * 1024), r1 = *(const v4u*)(rp + (KS + k) * 1024);
      v4u x0 = *(const v4u*)(xs + k * 1024 + lane * 16), x1 = *(const v4u*)(xs + (KS + k) * 1024 + lane * 16);
      sx[0][0] = mfma32(g0, x0, sx[0][0]); sx[1][0] = mfma32(g1, x0, sx[1][0]);
      sx[0][1] = mfma32(g0, x1, sx[0][1]); sx[1][1] = mfma32(g1, x1, sx[1][1]);
      sm[0][0] = mfma32(r0, x0, sm[0][0]); sm[1][0] = mfma32(r1, x0, sm[1][0]);
      sm[0][1] = mfma32(r0, x1, sm[0][1]); sm[1][1] = mfma32(r1, x1, sm[1][1]);
    }
#pragma unroll
    for (int t = 0; t < 2; ++t) {
      const int tok = sig(t, rho);
      float v[8], w[8];
      float m = -3.0e38f;
#pragma unroll
      for (int j = 0; j < 8; ++j) {
        const int key = 8 * g + j;
        v[j] = (key <= tok) ? sx[j >> 2][t][j & 3] : -3.0e38f;
        w[j] = (g == a) ? sm[j >> 2][t][j & 3] : -3.0e38f;
        m = fmaxf(m, fmaxf(v[j], w[j]));
      }
      m = xg_max(m);
      float l = 0.f;
#pragma unroll
      for (int j = 0; j < 8; ++j) {
        v[j] = (8 * g + j <= tok) ? __builtin_amdgcn_exp2f(v[j] - m) : 0.f;
        w[j] = (g == a) ? __builtin_amdgcn_exp2f(w[j] - m) : 0.f;
        l += v[j] + w[j];
      }
      l = xg_sum(l);
      const float il = 1.f / l;
      v4u px = {pk2(v[0] * il, v[1] * il), pk2(v[2] * il, v[3] * il), pk2(v[4] * il, v[5] * il), pk2(v[6] * il, v[7] * il)};
      v4u pm = {pk2(w[0] * il, w[1] * il), pk2(w[2] * il, w[3] * il), pk2(w[4] * il, w[5] * il), pk2(w[6] * il, w[7] * il)};
      *(v4u*)(ps + ((h * 2 + t) * 2 + 0) * 1024 + lane * 16) = px;
      *(v4u*)(ps + ((h * 2 + t) * 2 + 1) * 1024 + lane * 16) = pm;
    }
  }
  __syncthreads();
  if (wave < 7) {
    f32x4 acc[4][2];
#pragma unroll
    for (int c = 0; c < 4; ++c) { acc[c][0] = (f32x4){0.f, 0.f, 0.f, 0.f}; acc[c][1] = (f32x4){0.f, 0.f, 0.f, 0.f}; }
    const unsigned char* up = wb + OFF_UT + (size_t)s * SEGB + (size_t)(wave * 4) * 1024 + lane * 16;
    const unsigned char* vp = wb + OFF_VT + (size_t)grp * SEGB + (size_t)(wave * 4) * 1024 + lane * 16;
#pragma unroll 2
    for (int h = 0; h < NH; ++h) {
      v4u px0 = *(const v4u*)(ps + ((h * 2 + 0) * 2 + 0) * 1024 + lane * 16), pm0 = *(const v4u*)(ps + ((h * 2 + 0) * 2 + 1) * 1024 + lane * 16);
      v4u px1 = *(const v4u*)(ps + ((h * 2 + 1) * 2 + 0) * 1024 + lane * 16), pm1 = *(const v4u*)(ps + ((h * 2 + 1) * 2 + 1) * 1024 + lane * 16);
#pragma unroll
      for (int c = 0; c < 4; ++c) {
        v4u uf = *(const v4u*)(up + (size_t)(h * CT + c) * 1024), vf = *(const v4u*)(vp + (size_t)(h * CT + c) * 1024);
        acc[c][0] = mfma32(uf, px0, acc[c][0]); acc[c][1] = mfma32(uf, px1, acc[c][1]);
        acc[c][0] = mfma32(vf, pm0, acc[c][0]); acc[c][1] = mfma32(vf, pm1, acc[c][1]);
      }
    }
#pragma unroll
    for (int c = 0; c < 4; ++c)
#pragma unroll
      for (int t = 0; t < 2; ++t) {
        const int tok = s * 32 + sig(t, rho);
        v2u o = {pk2(acc[c][t][0], acc[c][t][1]), pk2(acc[c][t][2], acc[c][t][3])};
        *(v2u*)(in.out + (size_t)tok * 896 + 448 + (wave * 4 + c) * 16 + 4 * g) = o;
      }
  }
}

DEV float row16_max(float v) {
  v = fmaxf(v, dppf<DPP_XOR1>(v)); v = fmaxf(v, dppf<DPP_XOR2>(v));
  v = fmaxf(v, dppf<DPP_ROR4>(v)); v = fmaxf(v, dppf<DPP_ROR8>(v)); return v;
}
DEV float row16_sum(float v) {
  v += dppf<DPP_XOR1>(v); v += dppf<DPP_XOR2>(v); v += dppf<DPP_ROR4>(v); v += dppf<DPP_ROR8>(v); return v;
}
constexpr int A1_SPLIT = 8;
__global__ void __launch_bounds__(512, 2) k_a1(In in) {
  extern __shared__ __attribute__((aligned(16))) unsigned char lds[];
  unsigned char* wb = ws_base(in.ws);
  const int tid = threadIdx.x, lane = tid & 63, wave = rfl(tid >> 6);
  const int h = blockIdx.x % NH, part = blockIdx.x / NH;
  const int rho = lane & 15, g = lane >> 4;
  for (int i = tid; i < 1792; i += 512) {
    *(v4u*)(lds + i * 16) = *(const v4u*)(wb + OFF_WKF + (size_t)h * 28672 + i * 16);
    *(v4u*)(lds + 28672 + i * 16) = *(const v4u*)(wb + OFF_WVF + (size_t)h * 28672 + i * 16);
  }
  __syncthreads();
  f32x4 kv[4][2];
#pragma unroll
  for (int a = 0; a < 4; ++a) { kv[a][0] = (f32x4){0.f, 0.f, 0.f, 0.f}; kv[a][1] = (f32x4){0.f, 0.f, 0.f, 0.f}; }
  constexpr int CH = NSEG / A1_SPLIT / 8;
  for (int c = 0; c < CH; ++c) {
    const int s = (part * 8 + wave) * CH + c;
    const unsigned char* xp = wb + OFF_XAFR + (size_t)s * 28672 + lane * 16;
    f32x4 pk_[2][2], vv[2][2];
#pragma unroll
    for (int t = 0; t < 2; ++t)
#pragma unroll
      for (int b = 0; b < 2; ++b) { pk_[t][b] = (f32x4){0.f, 0.f, 0.f, 0.f}; vv[t][b] = (f32x4){0.f, 0.f, 0.f, 0.f}; }
#pragma unroll 2
    for (int k = 0; k < KS; ++k) {
      v4u x0 = *(const v4u*)(xp + k * 1024), x1 = *(const v4u*)(xp + (KS + k) * 1024);
      v4u k0 = *(const v4u*)(lds + k * 1024 + lane * 16), k1 = *(const v4u*)(lds + (KS + k) * 1024 + lane * 16);
      v4u v0 = *(const v4u*)(lds + 28672 + k * 1024 + lane * 16), v1 = *(const v4u*)(lds + 28672 + (KS + k) * 1024 + lane * 16);
      pk_[0][0] = mfma32(x0, k0, pk_[0][0]); pk_[0][1] = mfma32(x0, k1, pk_[0][1]);
      pk_[1][0] = mfma32(x1, k0, pk_[1][0]); pk_[1][1] = mfma32(x1, k1, pk_[1][1]);
      vv[0][0] = mfma32(x0, v0, vv[0][0]); vv[0][1] = mfma32(x0, v1, vv[0][1]);
      vv[1][0] = mfma32(x1, v0, vv[1][0]); vv[1][1] = mfma32(x1, v1, vv[1][1]);
    }
    const bool pad = rho >= 12;
    f32x4 pp[2][2], pn[2][2];
#pragma unroll
    for (int t = 0; t < 2; ++t)
#pragma unroll
      for (int r = 0; r < 4; ++r) {
        const float a0 = pk_[t][0][r], a1 = pk_[t][1][r];
        const float mx = row16_max(pad ? a0 : fmaxf(a0, a1));
        const float mn = -row16_max(pad ? -a0 : fmaxf(-a0, -a1));
        const float ep0 = __builtin_amdgcn_exp2f(a0 - mx), ep1 = pad ? 0.f : __builtin_amdgcn_exp2f(a1 - mx);
        const float en0 = __builtin_amdgcn_exp2f(mn - a0), en1 = pad ? 0.f : __builtin_amdgcn_exp2f(mn - a1);
        const float ip = 1.f / row16_sum(ep0 + ep1), in_ = 1.f / row16_sum(en0 + en1);
        pp[t][0][r] = ep0 * ip; pp[t][1][r] = ep1 * ip; pn[t][0][r] = en0 * in_; pn[t][1][r] = en1 * in_;
      }
    if (rho == 12) { vv[0][1] = (f32x4){1.f, 1.f, 1.f, 1.f}; vv[1][1] = (f32x4){1.f, 1.f, 1.f, 1.f}; }
    const v4u vb0 = pack8(vv[0][0], vv[1][0]), vb1 = pack8(vv[0][1], vv[1][1]);
    const v4u fa0 = pack8(pp[0][0], pp[1][0]), fa1 = pack8(pp[0][1], pp[1][1]), fa2 = pack8(pn[0][0], pn[1][0]), fa3 = pack8(pn[0][1], pn[1][1]);
    kv[0][0] = mfma32(fa0, vb0, kv[0][0]); kv[0][1] = mfma32(fa0, vb1, kv[0][1]);
    kv[1][0] = mfma32(fa1, vb0, kv[1][0]); kv[1][1] = mfma32(fa1, vb1, kv[1][1]);
    kv[2][0] = mfma32(fa2, vb0, kv[2][0]); kv[2][1] = mfma32(fa2, vb1, kv[2][1]);
    kv[3][0] = mfma32(fa3, vb0, kv[3][0]); kv[3][1] = mfma32(fa3, vb1, kv[3][1]);
  }
  __syncthreads();
  float* red = (float*)lds;
#pragma unroll
  for (int a = 0; a < 4; ++a)
#pragma unroll
    for (int b = 0; b < 2; ++b) *(f32x4*)(red + ((wave * 8 + a * 2 + b) * 64 + lane) * 4) = kv[a][b];
  __syncthreads();
  {
    f32x4 sum = {0.f, 0.f, 0.f, 0.f};
#pragma unroll
    for (int w = 0; w < 8; ++w) sum += *(const f32x4*)(red + (w * 8 * 64 + tid) * 4);
    *(f32x4*)(wb + OFF_KVP + ((size_t)(part * NH + h) * 512 + tid) * 16) = sum;
  }
}

__global__ void __launch_bounds__(512, 2) k_a2(In in) {
  extern __shared__ __attribute__((aligned(16))) unsigned char lds[];
  unsigned char* wb = ws_base(in.ws);
  const int tid = threadIdx.x, lane = tid & 63, wave = rfl(tid >> 6);
  const int rho = lane & 15, g = lane >> 4;
  for (int i = tid; i < NH * 4 * 64; i += 512) {
    const int l = i & 63, f = i >> 6, b = f & 1, sg = (f >> 1) & 1, h = f >> 2;
    f32x4 lo = {0.f, 0.f, 0.f, 0.f}, hi = {0.f, 0.f, 0.f, 0.f};
    for (int p = 0; p < A1_SPLIT; ++p) {
      const unsigned char* src = wb + OFF_KVP + (size_t)(p * NH + h) * 8192;
      lo += *(const f32x4*)(src + (((sg * 2 + 0) * 2 + b) * 64 + l) * 16);
      hi += *(const f32x4*)(src + (((sg * 2 + 1) * 2 + b) * 64 + l) * 16);
    }
    *(v4u*)(lds + i * 16) = pack8(lo, hi);
  }
  __syncthreads();
  const int tile = blockIdx.x * 8 + wave, s = tile >> 1, tt = tile & 1;
  v4u X[KS];
#pragma unroll
  for (int k = 0; k < KS; ++k) X[k] = *(const v4u*)(wb + OFF_XAFR + ((size_t)(s * 2 + tt) * KS + k) * 1024 + lane * 16);
  f32x4 acc[CT];
#pragma unroll
  for (int n = 0; n < CT; ++n) acc[n] = (f32x4){0.f, 0.f, 0.f, 0.f};
  for (int h = 0; h < NH; ++h) {
    const unsigned char* wq = wb + OFF_WQF + (size_t)h * 28672 + lane * 16;
    f32x4 p0 = {0.f, 0.f, 0.f, 0.f}, p1 = {0.f, 0.f, 0.f, 0.f};
#pragma unroll
    for (int k = 0; k < KS; ++k) {
      p0 = mfma32(*(const v4u*)(wq + k * 1024), X[k], p0);
      p1 = mfma32(*(const v4u*)(wq + (KS + k) * 1024), X[k], p1);
    }
    const bool pad = g == 3;
    float mx = fmaxf(fmaxf(p0[0], p0[1]), fmaxf(p0[2], p0[3])), mn = fminf(fminf(p0[0], p0[1]), fminf(p0[2], p0[3]));
    if (!pad) { mx = fmaxf(mx, fmaxf(fmaxf(p1[0], p1[1]), fmaxf(p1[2], p1[3]))); mn = fminf(mn, fminf(fminf(p1[0], p1[1]), fminf(p1[2], p1[3]))); }
    mx = xg_max(mx); mn = -xg_max(-mn);
    f32x4 ep0, ep1, en0, en1;
    float sp = 0.f, sn = 0.f;
#pragma unroll
    for (int r = 0; r < 4; ++r) {
      ep0[r] = __builtin_amdgcn_exp2f(p0[r] - mx); ep1[r] = pad ? 0.f : __builtin_amdgcn_exp2f(p1[r] - mx);
      en0[r] = __builtin_amdgcn_exp2f(mn - p0[r]); en1[r] = pad ? 0.f : __builtin_amdgcn_exp2f(mn - p1[r]);
      sp += ep0[r] + ep1[r]; sn += en0[r] + en1[r];
    }
    const float ip = 1.f / xg_sum(sp), in_ = 1.f / xg_sum(sn);
    const v4u bp = pack8(ep0 * ip, ep1 * ip), bn = pack8(en0 * in_, en1 * in_);
    const unsigned char* kvp = lds + h * 4096 + lane * 16;
    f32x4 n0 = {0.f, 0.f, 0.f, 0.f}, n1 = {0.f, 0.f, 0.f, 0.f};
    n0 = mfma32(*(const v4u*)(kvp), bp, n0); n1 = mfma32(*(const v4u*)(kvp + 1024), bp, n1);
    n0 = mfma32(*(const v4u*)(kvp + 2048), bn, n0); n1 = mfma32(*(const v4u*)(kvp + 3072), bn, n1);
    const float den = xg_sum(pad ? n1[0] : 0.f) + 1e-6f;
    const float id = 1.f / den;
    if (pad) n1 = (f32x4){0.f, 0.f, 0.f, 0.f};
    const v4u ob = pack8(n0 * id, n1 * id);
    const unsigned char* wo = wb + OFF_WOF + (size_t)h * CT * 1024 + lane * 16;
#pragma unroll
    for (int n = 0; n < CT; ++n) acc[n] = mfma32(*(const v4u*)(wo + n * 1024), ob, acc[n]);
  }
  const int tok = s * 32 + sig(tt, rho);
#pragma unroll
  for (int np = 0; np < KS; ++np)
    *(v4u*)(in.out + (size_t)tok * 896 + np * 32 + 8 * g) = pack8(acc[2 * np], acc[2 * np + 1]);
}

extern "C" void kernel_launch(void* const* d_in, const int* in_sizes, int n_in,
                              void* d_out, int out_size, void* d_ws, size_t ws_size,
                              hipStream_t stream) {
  In in;
  in.Ax = (const u16*)d_in[0]; in.Awqkv = (const u16*)d_in[1]; in.Awphi = (const u16*)d_in[2]; in.Awo = (const u16*)d_in[3];
  in.Bx = (const u16*)d_in[4]; in.Bmem0 = (const u16*)d_in[5]; in.Wq = (const u16*)d_in[6]; in.Wk = (const u16*)d_in[7];
  in.Wv = (const u16*)d_in[8]; in.Wo = (const u16*)d_in[9];
  in.out = (u16*)d_out; in.ws = d_ws;
  hipFuncSetAttribute((const void*)k_gu, hipFuncAttributeMaxDynamicSharedMemorySize, 114688);
  hipFuncSetAttribute((const void*)k_rec, hipFuncAttributeMaxDynamicSharedMemorySize, 98304);
  hipFuncSetAttribute((const void*)k_xrow, hipFuncAttributeMaxDynamicSharedMemorySize, 94208);
  hipFuncSetAttribute((const void*)k_a1, hipFuncAttributeMaxDynamicSharedMemorySize, 65536);
  hipFuncSetAttribute((const void*)k_a2, hipFuncAttributeMaxDynamicSharedMemorySize, 65536);
  k_probe<<<96, 256, 0, stream>>>(in);
  k_prep<<<768, 256, 0, stream>>>(in);
  k_gu<<<224, 512, 114688, stream>>>(in, 0);
  k_a1<<<NH * A1_SPLIT, 512, 65536, stream>>>(in);
  k_a2<<<16, 512, 65536, stream>>>(in);
  k_rec<<<256, 512, 98304, stream>>>(in);
  k_mfr<<<16 * 2 * KS * 64 / 256, 256, 0, stream>>>(in);
  k_gu<<<224, 512, 114688, stream>>>(in, 1);
  k_xrow<<<NSEG, 512, 94208, stream>>>(in);
}
```

```cpp
#include <hip/hip_runtime.h>
#include <hip/hip_bf16.h>

typedef unsigned short u16;
typedef __bf16 bf16x8 __attribute__((ext_vector_type(8)));
typedef __bf16 bf16x2 __attribute__((ext_vector_type(2)));
typedef short s16x4 __attribute__((ext_vector_type(4)));
typedef float f32x4 __attribute__((ext_vector_type(4)));
typedef unsigned v4u __attribute__((ext_vector_type(4)));
typedef unsigned v2u __attribute__((ext_vector_type(2)));
typedef unsigned v8u __attribute__((ext_vector_type(8)));
typedef __bf16 bf16x16 __attribute__((ext_vector_type(16)));
#define DEV __device__ __forceinline__
#define LAS __attribute__((address_space(3)))
#define GAS __attribute__((address_space(1)))

constexpr int DM = 448, NH = 16, EH = 256, NSEG = 64, KS = 14, CT = 28;
constexpr size_t MB = 1u << 20;
constexpr size_t OFF_XCH = 0;
constexpr size_t XCH_BYTES = 6 * MB;
constexpr int XCH_PAIRS_USED = 316;
constexpr size_t OFF_CTRL = 6 * MB;
constexpr size_t OFF_AFR = 8 * MB;
constexpr size_t OFF_BTFR = 16 * MB;
constexpr size_t OFF_XFR = 24 * MB;
constexpr size_t OFF_WQF = 26 * MB;
constexpr size_t OFF_WKF = 27 * MB;
constexpr size_t OFF_WVF = 28 * MB;
constexpr size_t OFF_WOF = 29 * MB;
constexpr size_t OFF_KVF = 30 * MB;
constexpr size_t OFF_DYN = 32 * MB;
constexpr unsigned DY_GT = 0;
constexpr unsigned DY_UT = 32u << 20;
constexpr unsigned DY_RC = 64u << 20;
constexpr unsigned DY_VC = 72u << 20;
constexpr unsigned DY_PX = 80u << 20;
constexpr unsigned DY_PM = 84u << 20;
constexpr unsigned DY_KV = 86u << 20;
constexpr unsigned DY_KVP = 87u << 20;
constexpr unsigned DYN_BYTES = 88u << 20;
constexpr unsigned SEGB = NH * CT * 1024;
constexpr unsigned OOB = 0xFFFFFF00u;
constexpr int SPIN_BUDGET = 1 << 20;
constexpr int CT_VOTE = 0;
constexpr int CT_TICK = 4096;
constexpr int CT_ABORT = 8192;
constexpr int CT_STEP = 8448;
constexpr int CT_ACNT = 8704;
constexpr int CT_Q1 = 8960;
constexpr int CT_Q2 = 9216;
constexpr int CT_HCNT = 10240;
constexpr int CT_PROG = 16384;
constexpr int CT_Q3 = 9472;
constexpr int CT_DONE = 12544;
constexpr int CT_XCNT = 32768;
constexpr int CTRL_BYTES = 40960;

DEV unsigned char* ws_base(void* ws) { const size_t a = (size_t)ws; return (unsigned char*)ws + (((a + (2 * MB - 1)) & ~(size_t)(2 * MB - 1)) - a); }
DEV unsigned pk2(float a, float b) { bf16x2 v = {(__bf16)a, (__bf16)b}; return __builtin_bit_cast(unsigned, v); }
DEV f32x4 mfma32(v4u a, v4u b, f32x4 c) { return __builtin_amdgcn_mfma_f32_16x16x32_bf16(__builtin_bit_cast(bf16x8, a), __builtin_bit_cast(bf16x8, b), c, 0, 0, 0); }
DEV f32x4 smfma64(v4u a, v8u b, f32x4 c, int idx) { return __builtin_amdgcn_smfmac_f32_16x16x64_bf16(__builtin_bit_cast(bf16x8, a), __builtin_bit_cast(bf16x16, b), c, idx, 0, 0); }
DEV v8u cat8(v4u a, v4u b) { return __builtin_shufflevector(a, b, 0, 1, 2, 3, 4, 5, 6, 7); }
DEV v4u pack8(f32x4 a, f32x4 b) { v4u r; r.x = pk2(a[0], a[1]); r.y = pk2(a[2], a[3]); r.z = pk2(b[0], b[1]); r.w = pk2(b[2], b[3]); return r; }
DEV int sig(int tau, int rho) { return 8 * (rho >> 2) + 4 * tau + (rho & 3); }
DEV unsigned xcc_id() { return (unsigned)__builtin_amdgcn_s_getreg(20 | (3 << 11)) & 0xfu; }
DEV int rfl(int x) { return __builtin_amdgcn_readfirstlane(x); }
DEV float f_as(unsigned x) { return __builtin_bit_cast(float, x); }
DEV unsigned u_as(float x) { return __builtin_bit_cast(unsigned, x); }
template <int CTRL> DEV float dppf(float x) { return f_as((unsigned)__builtin_amdgcn_update_dpp(0, (int)u_as(x), CTRL, 0xf, 0xf, true)); }
template <int CTRL> DEV unsigned dppu(unsigned x) { return (unsigned)__builtin_amdgcn_update_dpp(0, (int)x, CTRL, 0xf, 0xf, true); }
constexpr int DPP_ROR8 = 0x128, DPP_ROR4 = 0x124, DPP_XOR1 = 0xB1, DPP_XOR2 = 0x4E;
DEV float vmax(float a, float b) { return __builtin_elementwise_maximum(a, b); }
DEV float xg_max(float m) {
  auto a = __builtin_amdgcn_permlane32_swap(u_as(m), u_as(m), false, false);
  m = vmax(f_as(a[0]), f_as(a[1]));
  auto b = __builtin_amdgcn_permlane16_swap(u_as(m), u_as(m), false, false);
  return vmax(f_as(b[0]), f_as(b[1]));
}
DEV float xg_sum(float m) {
  auto a = __builtin_amdgcn_permlane32_swap(u_as(m), u_as(m), false, false);
  m = f_as(a[0]) + f_as(a[1]);
  auto b = __builtin_amdgcn_permlane16_swap(u_as(m), u_as(m), false, false);
  return f_as(b[0]) + f_as(b[1]);
}
typedef __amdgpu_buffer_rsrc_t rsrc_t;
DEV rsrc_t mk_rsrc(void* p, unsigned bytes) { return __builtin_amdgcn_make_buffer_rsrc(p, 0, (int)bytes, 0x00020000); }
DEV v4u ld_sc1(rsrc_t r, unsigned off) { return __builtin_amdgcn_raw_buffer_load_b128(r, off, 0, 16); }
DEV v4u ld_sc1s(rsrc_t r, unsigned voff, unsigned soff) { return __builtin_amdgcn_raw_buffer_load_b128(r, voff, soff, 16); }
DEV v4u ld_s(rsrc_t r, unsigned voff, unsigned soff) { return __builtin_amdgcn_raw_buffer_load_b128(r, voff, soff, 0); }
DEV void lds_dma(rsrc_t r, unsigned char* ldsp, unsigned voff, unsigned soff) { __builtin_amdgcn_raw_ptr_buffer_load_lds(r, (LAS void*)ldsp, 16, voff, soff, 0, 0); }
DEV void st_sc1(rsrc_t r, unsigned off, v4u v) { __builtin_amdgcn_raw_buffer_store_b128(v, r, off, 0, 16); }
DEV unsigned ld32_sc1(rsrc_t r, unsigned off) { return __builtin_amdgcn_raw_buffer_load_b32(r, off, 0, 16); }
DEV void st32_sc1(rsrc_t r, unsigned off, unsigned v) { __builtin_amdgcn_raw_buffer_store_b32(v, r, off, 0, 16); }
DEV unsigned long long memtime() { unsigned long long t; asm volatile("s_memtime %0\n\ts_waitcnt lgkmcnt(0)" : "=s"(t) :: "memory"); return t; }
#define ODELAY 0
#define MDELAY 12
#define TA1 1100u
#define TA2 800u
#define TA3 2850u
#define TDX 350u
#define TUNE_LOAD
DEV unsigned now32() { return (unsigned)__builtin_amdgcn_s_memtime(); }
DEV void wait_abs(unsigned t) { while ((int)(now32() - t) < 0) {} }
DEV v4u nosentu(v4u r) { v4u o = {min(r.x, 0xFFFFFFFEu), min(r.y, 0xFFFFFFFEu), min(r.z, 0xFFFFFFFEu), min(r.w, 0xFFFFFFFEu)}; return o; }
DEV v4u nosent(f32x4 r) { v4u o = {min(u_as(r[0]), 0xFFFFFFFEu), min(u_as(r[1]), 0xFFFFFFFEu), min(u_as(r[2]), 0xFFFFFFFEu), min(u_as(r[3]), 0xFFFFFFFEu)}; return o; }
#define BARRIER() asm volatile("s_waitcnt lgkmcnt(0)\n\ts_barrier" ::: "memory")
#define DRAIN_BARRIER() asm volatile("s_waitcnt vmcnt(0) lgkmcnt(0)\n\ts_barrier" ::: "memory")
#define Z4 ((f32x4){0.f, 0.f, 0.f, 0.f})
#define PIN2(a, b) asm volatile("" : "+v"(a), "+v"(b) :: "memory")

#define STAMPV(role, s, wave, k) do {} while (0)
#define STAMPDUMP(role) do {} while (0)
#define STAMPX(s, wave, k, val) do {} while (0)
#define STAMPG(slot, k) do {} while (0)
#define STAMPC(slot, k) do {} while (0)
#define STAMPP(k) do {} while (0)
#define STAMPW(slot, k) do {} while (0)

#define CP_DECL
#define CP(k) do {} while (0)
#define CP_DUMP(role) do {} while (0)
#define CPV(x) do {} while (0)
struct In {
  const u16 *Ax, *Awqkv, *Awphi, *Awo, *Bx, *Bmem0, *Wq, *Wk, *Wv, *Wo;
  u16* out; void* ws;
};

DEV v4u ld16_a8(const u16* p) { v2u a = *(const v2u*)p, b = *(const v2u*)(p + 4); return (v4u){a.x, a.y, b.x, b.y}; }

DEV void prep_parta_unit(const In& in, unsigned char* wb, int u, int lane) {
  const int rho = lane & 15, g = lane >> 4;
  if (u < 3 * NH * KS) {
    const int ks = u % KS, h = (u / KS) % NH, which = u / (KS * NH);
    v4u a[2], b[2];
#pragma unroll
    for (int t = 0; t < 2; ++t) {
      const int k = 32 * ks + sig(t, rho);
      const u16* src = in.Awqkv + (size_t)k * 1344 + which * 448 + h * 28 + 8 * g;
      v2u lo = *(const v2u*)src, hi = {0u, 0u};
      if (g < 3) hi = *(const v2u*)(src + 4);
      a[t] = (v4u){lo.x, lo.y, hi.x, hi.y};
    }
#pragma unroll
    for (int t = 0; t < 2; ++t) {
      const int f = sig(t, rho);
      unsigned e[8];
#pragma unroll
      for (int j = 0; j < 8; ++j) {
        const int d = 8 * g + j;
        unsigned v = 0;
        if (d < 28 && f < 28) v = which == 2 ? (d == f ? 0x3F80u : 0u) : (unsigned)in.Awphi[(h * 28 + d) * 28 + f];
        e[j] = v;
      }
      b[t] = (v4u){e[0] | (e[1] << 16), e[2] | (e[3] << 16), e[4] | (e[5] << 16), e[6] | (e[7] << 16)};
    }
    const float sc = which == 2 ? 1.f : 1.4426950408889634f;
    unsigned char* dst = wb + (which == 0 ? OFF_WQF : (which == 1 ? OFF_WKF : OFF_WVF));
#pragma unroll
    for (int t = 0; t < 2; ++t) {
      f32x4 d0 = mfma32(a[0], b[t], Z4), d1 = mfma32(a[1], b[t], Z4);
      *(v4u*)(dst + ((size_t)(h * 2 + t) * KS + ks) * 1024 + lane * 16) = pack8(d0 * sc, d1 * sc);
    }
  } else {
    const int v = u - 3 * NH * KS, np = v % KS, h = v / KS;
    v4u a[2];
#pragma unroll
    for (int t = 0; t < 2; ++t) {
      const int d = sig(t, rho);
      a[t] = (v4u){0u, 0u, 0u, 0u};
      if (d < 28) a[t] = *(const v4u*)(in.Awo + (size_t)(h * 28 + d) * DM + np * 32 + 8 * g);
    }
#pragma unroll
    for (int t = 0; t < 2; ++t) {
      const int n = sig(t, rho) - 8 * g;
      unsigned e[4] = {0u, 0u, 0u, 0u};
#pragma unroll
      for (int j = 0; j < 4; ++j) e[j] = (n == 2 * j ? 0x3F80u : 0u) | (n == 2 * j + 1 ? 0x3F800000u : 0u);
      const v4u b = {e[0], e[1], e[2], e[3]};
      f32x4 d0 = mfma32(a[0], b, Z4), d1 = mfma32(a[1], b, Z4);
      *(v4u*)(wb + OFF_WOF + (size_t)(h * CT + np * 2 + t) * 1024 + lane * 16) = pack8(d0, d1);
    }
  }
}

#define PAUX 16
#define PST(r, off, v) __builtin_amdgcn_raw_buffer_store_b128(v, r, off, 0, PAUX)
constexpr int PREP_GRID = 256 + 128 + 37;
__global__ void __launch_bounds__(448, 4) k_prep(In in) {
  extern __shared__ __attribute__((aligned(16))) unsigned char lds[];
  unsigned char* wb = ws_base(in.ws);
  const int tid = threadIdx.x, lane = tid & 63, wave = rfl(tid >> 6), b = blockIdx.x;
  const int rho = lane & 15, g = lane >> 4;
  STAMPP(0);
  if (b >= 384) {
    const int pi = (b - 384) * 7 + wave;
    if (lane == 0 && pi < 256) {
      const int pair = (pi * 3) >> 1;
      unsigned* p0 = (unsigned*)(wb + OFF_XCH + (size_t)pair * 16384 + 8064);
      unsigned* p1 = p0 + 2048;
      unsigned t0 = ~0u, t1 = ~0u;
      for (int rep = 0; rep < 3; ++rep) {
        unsigned v, v2, z = 0;
        unsigned long long a = memtime();
        asm volatile("global_atomic_or %0, %1, %2, off sc0\n\ts_waitcnt vmcnt(0)" : "=v"(v) : "v"(p0), "v"(z) : "memory");
        unsigned long long bb = memtime();
        asm volatile("global_atomic_or %0, %1, %2, off sc0\n\ts_waitcnt vmcnt(0)" : "=v"(v2) : "v"(p1), "v"(z) : "memory");
        unsigned long long c = memtime();
        t0 = min(t0, (unsigned)(bb - a)); t1 = min(t1, (unsigned)(c - bb));
      }
      const int vote = (t1 < t0) ^ (xcc_id() >= 4);
      const unsigned d = t0 > t1 ? t0 - t1 : t1 - t0;
      ((int*)(wb + OFF_CTRL + CT_VOTE))[pi] = (pair >> 7) * 4 + (d > 100 ? vote : 2);
    }
    STAMPP(3);
    return;
  }
  if (b >= 256) { prep_parta_unit(in, wb, (b - 256) * 7 + wave, lane); STAMPP(3); return; }
  const int mat = (b & 7) * 4 + (b >> 6), sub = (b >> 3) & 7;
  const int m = mat >> 4, h = mat & 15, rh = sub >> 2, cq = sub & 3;
  const rsrc_t rw = mk_rsrc(wb, (unsigned)OFF_DYN);
  const rsrc_t rq = mk_rsrc((void*)(m == 0 ? in.Wq : in.Wo), 448u * 4096u * 2u);
  const rsrc_t rp_ = mk_rsrc((void*)(m == 0 ? in.Wk : in.Wv), 448u * 4096u * 2u);
  const int rp = rh * 7 + wave;
  unsigned qv[8];
  if (m == 0) {
    const int T = cq * 7 + wave, k = 32 * (T >> 1) + sig(T & 1, rho);
#pragma unroll
    for (int es = 0; es < 8; ++es) qv[es] = (unsigned)(k * 4096 + h * EH + es * 32 + g * 8) * 2u;
  } else {
#pragma unroll
    for (int i = 0; i < 8; ++i) {
      const int n = ((i >> 2) * 28 + wave * 4 + (i & 3)) * 64 + lane, e = n / 14, ch = n % 14;
      qv[i] = (unsigned)((h * EH + e) * DM + cq * 112 + ch * 8) * 2u;
    }
  }
  const unsigned pv0 = (unsigned)((32 * rp + sig(0, rho)) * 4096 + h * EH + g * 8) * 2u, pv1 = pv0 + 4u * 4096u * 2u;
  v4u pa[2][8];
#pragma unroll
  for (int hf = 0; hf < 2; ++hf) {
#pragma unroll
    for (int i = 0; i < 4; ++i) {
      const int slot = m == 0 ? wave * 8 + hf * 4 + i : hf * 28 + wave * 4 + i;
      lds_dma(rq, lds + slot * 1024, qv[hf * 4 + i], 0u);
    }
#pragma unroll
    for (int i = 0; i < 4; ++i) { pa[0][hf * 4 + i] = ld_s(rp_, pv0, (hf * 4 + i) * 64u); pa[1][hf * 4 + i] = ld_s(rp_, pv1, (hf * 4 + i) * 64u); }
  }
  v4u xv;
  {
    const int j = b * 448 + tid, l = j & 63, f = j >> 6, cs = f % KS, tau = (f / KS) & 1, s = f / (2 * KS);
    xv = *(const v4u*)(in.Bx + (size_t)(s * 32 + sig(tau, l & 15)) * DM + cs * 32 + (l >> 4) * 8);
  }
  {
    const float sc = m ? 1.f : 0.0625f * 1.4426950408889634f;
    const unsigned dst = (unsigned)(m ? OFF_BTFR : OFF_AFR) + (unsigned)((h * CT + cq * 7) * KS + rp) * 1024u + lane * 16u;
    const unsigned troff = (unsigned)((8 * g + (rho >> 2)) * 224 + (rho & 3) * 8);
    DRAIN_BARRIER();
    STAMPP(1);
    if (m == 0) {
      v4u qb[2][8];
#pragma unroll
      for (int es = 0; es < 8; ++es) qb[0][es] = *(const v4u*)(lds + es * 1024 + lane * 16);
#pragma unroll
      for (int i = 0; i < 7; ++i) {
        f32x4 a0 = Z4, a1 = Z4;
#pragma unroll
        for (int es = 0; es < 8; ++es) {
          if (i + 1 < 7 && (es & 1) == 0) {
            qb[(i + 1) & 1][es] = *(const v4u*)(lds + ((i + 1) * 8 + es) * 1024 + lane * 16);
          }
          a0 = mfma32(pa[0][es], qb[i & 1][es], a0); a1 = mfma32(pa[1][es], qb[i & 1][es], a1);
          if (i + 1 < 7 && (es & 1) == 1) {
            qb[(i + 1) & 1][es] = *(const v4u*)(lds + ((i + 1) * 8 + es) * 1024 + lane * 16);
          }
        }
        PST(rw, dst + i * (KS * 1024u), pack8(a0 * sc, a1 * sc));
      }
    } else {
#pragma unroll
      for (int i = 0; i < 7; ++i) {
        f32x4 a0 = Z4, a1 = Z4;
#pragma unroll
        for (int es = 0; es < 8; ++es) {
          s16x4 lo = __builtin_amdgcn_ds_read_tr16_b64_v4i16((LAS s16x4*)(lds + troff + es * 32 * 224 + i * 32));
          s16x4 hi = __builtin_amdgcn_ds_read_tr16_b64_v4i16((LAS s16x4*)(lds + troff + (es * 32 + 4) * 224 + i * 32));
          v2u l2 = __builtin_bit_cast(v2u, lo), h2 = __builtin_bit_cast(v2u, hi);
          const v4u qb = {l2.x, l2.y, h2.x, h2.y};
          a0 = mfma32(pa[0][es], qb, a0); a1 = mfma32(pa[1][es], qb, a1);
        }
        PST(rw, dst + i * (KS * 1024u), pack8(a0, a1));
      }
    }
  }
  STAMPP(2);
  PST(rw, (unsigned)OFF_XFR + (unsigned)(b * 448 + tid) * 16u, xv);
  for (int o = b * 448 + tid; o < (CTRL_BYTES - CT_TICK) / 4; o += 256 * 448) *(unsigned*)(wb + OFF_CTRL + CT_TICK + o * 4) = 0u;
  for (unsigned o = (unsigned)(b * 448 + tid) * 16u; o < XCH_PAIRS_USED * 16384u; o += 256u * 448u * 16u) {
    v4u v = {~0u, ~0u, ~0u, ~0u};
    if (o < 16384) {
      const int w = (int)(o & 8191);
      if (w < 7168) {
        const int slot = w >> 7, cg = slot >> 1, rg = slot & 1, ii = (w >> 5) & 3, hf = (w >> 4) & 1;
        v = *(const v4u*)(in.Bmem0 + (rg * 4 + ii) * DM + cg * 16 + hf * 8);
        v.x = min(v.x, 0xFFFFFFFEu); v.y = min(v.y, 0xFFFFFFFEu); v.z = min(v.z, 0xFFFFFFFEu); v.w = min(v.w, 0xFFFFFFFEu);
      }
    }
    PST(rw, (unsigned)OFF_XCH + o, v);
  }
  {
    const rsrc_t rd = mk_rsrc(wb + OFF_DYN, DYN_BYTES);
    const v4u v = {~0u, ~0u, ~0u, ~0u};
    const unsigned t16 = (unsigned)(b * 448 + tid) * 16u;
    const unsigned RB = NH * KS * 512u;
    PST(rd, t16 < 2u * SEGB ? DY_GT + t16 : DY_UT + (t16 - 2u * SEGB), v);
    unsigned o = OOB;
    if (t16 < 2u * RB) o = DY_RC + 62u * RB + t16;
    else if (t16 < 4u * RB) o = DY_VC + 62u * RB + (t16 - 2u * RB);
    else if (t16 < 4u * RB + 65536u) o = DY_PX + 62u * 32768u + (t16 - 4u * RB);
    else if (t16 < 4u * RB + 81920u) o = DY_PM + 62u * 8192u + (t16 - 4u * RB - 65536u);
    PST(rd, o, v);
  }
  STAMPP(3);
}

DEV unsigned read_parity(const unsigned char* wb, int lane) {
  const int* v = (const int*)(wb + OFF_CTRL + CT_VOTE);
  int x[4];
#pragma unroll
  for (int k = 0; k < 4; ++k) x[k] = v[k * 64 + lane];
  unsigned par = 0;
#pragma unroll
  for (int p = 0; p < 3; ++p) {
    int n1 = 0, n0 = 0;
#pragma unroll
    for (int k = 0; k < 4; ++k) { n1 += __popcll(__ballot(x[k] == p * 4 + 1)); n0 += __popcll(__ballot(x[k] == p * 4)); }
    par |= (n1 > n0 ? 1u : 0u) << p;
  }
  return par;
}
DEV unsigned pair_off(unsigned par, int p) { return (unsigned)p * 16384u + (((par >> (p >> 7)) & 1u) << 13); }
DEV unsigned memx_off(unsigned par, int s) { return pair_off(par, s); }
DEV unsigned scx_off(unsigned par, int s, int rg, int h) { return pair_off(par, 64 + 4 * s + 2 * rg + (h >> 3)) + (h & 7) * 1024; }
DEV bool any_invalid(v4u v) { unsigned m = max(max(v.x, v.y), max(v.z, v.w)); return __any(m == 0xFFFFFFFFu); }
DEV v4u poll1(rsrc_t rx, unsigned off, int& budget) {
  v4u t;
#pragma nounroll
  do { asm volatile("" ::: "memory"); t = ld_sc1(rx, off); --budget; } while (any_invalid(t) && budget > 0);
  return t;
}
DEV unsigned simg_off(int c) {
  const int cg = c >> 4, rgi = (c >> 3) & 1, ii = (c >> 1) & 3, hf = c & 1;
  return (unsigned)((cg >> 2) * 1024 + (cg & 3) * 256 + (rgi * 4 + ii) * 32 + hf * 8);
}
DEV void simg_write(unsigned char* simg, unsigned off, v4u t) {
  *(v2u*)(simg + off) = (v2u){t.x, t.z};
  *(v2u*)(simg + off + 16) = (v2u){t.y, t.w};
}
DEV int wait_words(rsrc_t rc, unsigned off, int need, int& budget, bool sleep) {
  int mn;
  for (;;) {
    asm volatile("" ::: "memory");
    const int v = off == OOB ? 0x7fffffff : (int)ld32_sc1(rc, off);
    --budget;
    if (__all(v >= need) || budget <= 0) {
      mn = need;
      if (__all(v >= need + 16)) mn = need + 16; else if (__all(v >= need + 4)) mn = need + 4;
      break;
    }
    if (sleep) { __builtin_amdgcn_s_sleep(8); budget -= 4; }
  }
  return mn;
}

template <bool ISG>
DEV void gu_compute(const v4u (&W)[2][KS], const unsigned char* xb, v4u& r0, v4u& r1) {
  f32x4 a00 = Z4, a01 = Z4, a10 = Z4, a11 = Z4;
  v4u x0[KS], x1[KS];
#pragma unroll
  for (int k = 0; k < 2; ++k) { x0[k] = *(const v4u*)(xb + k * 1024); x1[k] = *(const v4u*)(xb + (KS + k) * 1024); }
  __builtin_amdgcn_sched_barrier(0);
#pragma unroll
  for (int k = 0; k < KS; ++k) {
    if (k + 2 < KS) { x0[k + 2] = *(const v4u*)(xb + (k + 2) * 1024); x1[k + 2] = *(const v4u*)(xb + (KS + k + 2) * 1024); }
    if (ISG) {
      a00 = mfma32(W[0][k], x0[k], a00); a10 = mfma32(W[1][k], x0[k], a10);
      a01 = mfma32(W[0][k], x1[k], a01); a11 = mfma32(W[1][k], x1[k], a11);
    } else {
      a00 = mfma32(x0[k], W[0][k], a00); a10 = mfma32(x0[k], W[1][k], a10);
      a01 = mfma32(x1[k], W[0][k], a01); a11 = mfma32(x1[k], W[1][k], a11);
    }
    __builtin_amdgcn_sched_barrier(0);
  }
  if (ISG) { r0 = pack8(a00, a10); r1 = pack8(a01, a11); } else { r0 = pack8(a00, a01); r1 = pack8(a10, a11); }
}

template <bool ISG>
DEV void gu_worker(unsigned char* wb, int w, unsigned char* lds, int tid) {
  const int lane = tid & 63, wave = rfl(tid >> 6), tp = wave & 3, sp = wave >> 2;
  const unsigned wfr = (unsigned)(ISG ? OFF_AFR : OFF_BTFR);
  const rsrc_t rw = mk_rsrc(wb, (unsigned)OFF_DYN);
  const rsrc_t rd = mk_rsrc(wb + OFF_DYN, DYN_BYTES), rc = mk_rsrc(wb + OFF_CTRL, CTRL_BYTES);
  const int T = 8 * (ISG ? w : w - 56) + 2 * tp;
  const int h = T / CT, tl = T % CT;
  auto stage = [&](int s, int buf) {
#pragma unroll
    for (int i = 0; i < 7; ++i)
      lds_dma(rw, lds + buf * 57344 + i * 8192 + wave * 1024, tid * 16u, (unsigned)OFF_XFR + (unsigned)s * 28672u + i * 8192u);
  };
  stage(0, 0);
  v4u W[2][KS];
#pragma unroll
  for (int t = 0; t < 2; ++t)
#pragma unroll
    for (int k = 0; k < KS; ++k) W[t][k] = ld_s(rw, lane * 16u, wfr + (unsigned)((T + t) * KS + k) * 1024u);
  const unsigned o0 = (ISG ? (unsigned)((h * 2 + 0) * KS + (tl >> 1)) : (unsigned)T) * 1024u + lane * 16u;
  const unsigned o1 = (ISG ? (unsigned)((h * 2 + 1) * KS + (tl >> 1)) : (unsigned)(T + 1)) * 1024u + lane * 16u;
#pragma unroll
  for (int k = 0; k < KS; ++k) PIN2(W[0][k], W[1][k]);
  STAMPG(128 + w, 8); STAMPC(128 + w, 9);
  int buf = 0;
  for (int s = 0; s < NSEG; s += 2) {
    if (s <= 4) asm volatile("s_waitcnt vmcnt(0) lgkmcnt(0)\n\ts_barrier" ::: "memory");
    else asm volatile("s_waitcnt vmcnt(2) lgkmcnt(0)\n\ts_barrier" ::: "memory");
    if (wave == 7) st32_sc1(rc, lane == 0 ? (unsigned)(CT_PROG + w * 128) : OOB, (unsigned)(s <= 4 ? s : s - 2));
    stage(s + 2 < NSEG ? s + 2 : s, buf ^ 1);
    v4u r0, r1;
    gu_compute<ISG>(W, lds + buf * 57344 + sp * 28672 + lane * 16, r0, r1);
    const unsigned ob = (ISG ? DY_GT : DY_UT) + (unsigned)(s + sp) * SEGB;
    if (s == 0) STAMPG(128 + w, 11);
    st_sc1(rd, ob + o0, nosentu(r0)); st_sc1(rd, ob + o1, nosentu(r1));
    buf ^= 1;
  }
  DRAIN_BARRIER();
  if (wave == 7) st32_sc1(rc, lane == 0 ? (unsigned)(CT_PROG + w * 128) : OOB, (unsigned)NSEG);
  STAMPG(128 + w, 5); STAMPC(128 + w, 10);
}

template <bool HASG>
DEV void role_S(unsigned char* wb, int h, int q, unsigned char* lds, int tid) {
  const int lane = tid & 63, wave = rfl(tid >> 6);
  TUNE_LOAD
  unsigned char* rimg = lds;
  unsigned char* mimg = lds + 4096;
  unsigned char* simg = lds + 12288;
  unsigned char* gring = lds + 20480;
  const rsrc_t rx = mk_rsrc(wb + OFF_XCH, (unsigned)XCH_BYTES);
  int budget = SPIN_BUDGET;
  const unsigned soff = simg_off(tid);
  if (wave == 7) {
    if (!HASG) { BARRIER(); return; }
    const rsrc_t rd = mk_rsrc(wb + OFF_DYN, DYN_BYTES), rc = mk_rsrc(wb + OFF_CTRL, CTRL_BYTES);
    const int w0 = (28 * h) >> 3, w1 = (28 * h + 27) >> 3;
    const unsigned poff = (w0 + lane <= w1) ? (unsigned)(CT_PROG + (w0 + lane) * 128) : OOB;
    const unsigned gsrc = DY_GT + (unsigned)((h * 2 + q) * KS) * 1024u;
    int known = 0;
    auto fetch = [&](int s) {
      if (known < s + 1) known = wait_words(rc, poff, s + 1, budget, false);
      v4u t[KS];
#pragma unroll
      for (int k = 0; k < KS; ++k) t[k] = ld_sc1s(rd, lane * 16u, gsrc + (unsigned)s * SEGB + k * 1024);
#pragma unroll
      for (int k = 0; k < KS; ++k) *(v4u*)(gring + (s & 1) * 14336 + k * 1024 + lane * 16) = t[k];
    };
    {
      poll1(rd, lane < 2 * KS ? gsrc + (lane / KS) * SEGB + (lane % KS) * 1024u : OOB, budget);
      v4u t[2 * KS];
      bool bad;
#pragma nounroll
      do {
        asm volatile("" ::: "memory");
        unsigned m = 0;
#pragma unroll
        for (int k = 0; k < 2 * KS; ++k) t[k] = ld_sc1s(rd, lane * 16u, gsrc + (k / KS) * SEGB + (k % KS) * 1024);
#pragma unroll
        for (int k = 0; k < 2 * KS; ++k) m = max(m, max(max(t[k].x, t[k].y), max(t[k].z, t[k].w)));
        bad = __any(m == 0xFFFFFFFFu);
        budget -= 8;
      } while (bad && budget > 0);
#pragma unroll
      for (int k = 0; k < 2 * KS; ++k) *(v4u*)(gring + k * 1024 + lane * 16) = t[k];
      STAMPW(h * 4 + q, 12);
    }
    BARRIER();
    for (int s = 0; s < NSEG; ++s) {
      if (s > 0 && s + 1 < NSEG - 1) fetch(s + 1);
      BARRIER();
      BARRIER();
    }
    return;
  }
  if (wave >= 4) {
    const unsigned par = read_parity(wb, lane);
    BARRIER();
    unsigned tanch = now32() - 100000u;
    for (int s = 0; s < NSEG; ++s) {
      wait_abs(tanch + TA3);
      STAMPV(h * 4 + q, s, wave, 2);
      const int b0 = budget;
      const v4u t = poll1(rx, memx_off(par, s) + tid * 16, budget);
      simg_write(simg, soff, t);
      *(v4u*)(mimg + tid * 16) = t;
      STAMPV(h * 4 + q, s, wave, 0);
      STAMPX(s, wave, 1, b0 - budget);
      BARRIER();
      BARRIER();
      tanch = now32();
    }
    STAMPDUMP(h * 4 + q);
    return;
  }
  const int cp0 = q == 0 ? 0 : (q == 1 ? 3 : (q == 2 ? 6 : 10));
  const int i = lane & 15, g = lane >> 4, i8 = i & 7;
  const unsigned char* fbase = mimg + (g >> 1) * 256 + (i8 >> 2) * 128 + (i8 & 3) * 32 + (g & 1) * 16;
  const bool act = i < 8;
  if (HASG && wave == 3) {
    const unsigned par = read_parity(wb, lane);
    BARRIER();
    const unsigned pl = (unsigned)(512 + q * 256 + (i & 3) * 64 + g * 16);
    unsigned tanch = now32() - 100000u;
    for (int s = 0; s < NSEG - 1; ++s) {
      v4u G[KS];
#pragma unroll
      for (int k = 0; k < KS; ++k) G[k] = *(const v4u*)(gring + (s & 1) * 14336 + k * 1024 + lane * 16);
      const unsigned pubo = act ? scx_off(par, s, i >> 2, h) + pl : OOB;
      wait_abs(tanch + TA3);
      const v4u t = poll1(rx, memx_off(par, s) + tid * 16, budget);
      simg_write(simg, soff, t);
      *(v4u*)(mimg + tid * 16) = t;
      BARRIER();
      f32x4 acc = Z4;
      v4u F[KS];
#pragma unroll
      for (int k = 0; k < 6; ++k) F[k] = *(const v4u*)(fbase + k * 512);
      __builtin_amdgcn_sched_barrier(0);
#pragma unroll
      for (int k = 0; k < KS; ++k) {
        if (k + 6 < KS) F[k + 6] = *(const v4u*)(fbase + (k + 6) * 512);
        acc = mfma32(G[k], F[k], acc);
        __builtin_amdgcn_sched_barrier(0);
      }
      st_sc1(rx, pubo, nosent(acc));
      STAMPV(h * 4 + q, s, wave, 2);
      BARRIER();
      tanch = now32();
    }
    {
      wait_abs(tanch + TA3);
      const v4u t = poll1(rx, memx_off(par, NSEG - 1) + tid * 16, budget);
      simg_write(simg, soff, t);
      *(v4u*)(mimg + tid * 16) = t;
      BARRIER();
      BARRIER();
    }
    STAMPDUMP(h * 4 + q);
    return;
  }
  constexpr int NW = HASG ? 3 : 4;
  const int cp = cp0 + wave;
  const rsrc_t rw = mk_rsrc(wb, (unsigned)OFF_DYN), rd = mk_rsrc(wb + OFF_DYN, DYN_BYTES);
  v8u W[2][7];
#pragma unroll
  for (int t = 0; t < 2; ++t)
#pragma unroll
    for (int k = 0; k < 7; ++k) {
      const unsigned o = (unsigned)OFF_AFR + (unsigned)((h * CT + 2 * cp + t) * KS + 2 * k) * 1024u;
      W[t][k] = cat8(ld_s(rw, lane * 16u, o), ld_s(rw, lane * 16u, o + 1024u));
    }
  __builtin_amdgcn_sched_barrier(0);
  const unsigned par = read_parity(wb, lane);
  const unsigned char* abase = simg + g * 256 + (2 * (i >> 2) + (i & 1)) * 32 + ((i >> 1) & 1) * 16;
  const int sidx = (i & 2) ? 0xEEEE : 0x4444;
  unsigned char* rwr = rimg + (wave * 32 + sig(0, i)) * 16 + g * 4;
  const unsigned trbase = (unsigned)((8 * g + (i >> 2)) * 16 + (i & 1) * 8);
  const unsigned pub_l = (unsigned)(q * 128 + (i & 3) * 32 + (g & 1) * 16);
  const bool pact = act && g < 2;
  const unsigned rlane = DY_RC + (unsigned)(h * KS + cp) * 512u + (unsigned)(g * 8 + i) * 16u;
  auto trfrag = [&](int p) {
    s16x4 lo = __builtin_amdgcn_ds_read_tr16_b64_v4i16((LAS s16x4*)(rimg + trbase + p * 512));
    s16x4 hi = __builtin_amdgcn_ds_read_tr16_b64_v4i16((LAS s16x4*)(rimg + trbase + p * 512 + 64));
    v2u l2 = __builtin_bit_cast(v2u, lo), h2 = __builtin_bit_cast(v2u, hi);
    return (v4u){l2.x, l2.y, h2.x, h2.y};
  };
  if (wave == 0) STAMPW(h * 4 + q, 11);
  BARRIER();
  CP_DECL
  unsigned tanch = now32() - 100000u;
  for (int s = 0; s < NSEG; ++s) {
    const unsigned pubo = (pact && s < NSEG - 1) ? scx_off(par, s, i >> 2, h) + pub_l : OOB;
    wait_abs(tanch + TA3);
    CP(0);
    const v4u t = poll1(rx, memx_off(par, s) + tid * 16, budget);
    CP(1);
    simg_write(simg, soff, t);
    *(v4u*)(mimg + tid * 16) = t;
    BARRIER();
    CP(2);
    f32x4 a0 = Z4, a1 = Z4;
    v4u F[7], Fc[NW];
#pragma unroll
    for (int k = 0; k < 4; ++k) F[k] = *(const v4u*)(abase + k * 1024);
    __builtin_amdgcn_sched_barrier(0);
#pragma unroll
    for (int k = 0; k < 7; ++k) {
      if (k + 4 < 7) F[k + 4] = *(const v4u*)(abase + (k + 4) * 1024);
      if (k >= 3 && k - 3 < NW) Fc[k - 3] = *(const v4u*)(fbase + (cp0 + k - 3) * 512);
      a0 = smfma64(F[k], W[0][k], a0, sidx); a1 = smfma64(F[k], W[1][k], a1, sidx);
      __builtin_amdgcn_sched_barrier(0);
    }
    STAMPV(h * 4 + q, s, wave, 0);
    *(unsigned*)(rwr) = pk2(a0[0] + a0[2], a0[1] + a0[3]);
    *(unsigned*)(rwr + 64) = pk2(a1[0] + a1[2], a1[1] + a1[3]);
    STAMPV(h * 4 + q, s, wave, 1);
    CP(3);
    BARRIER();
    CP(4);
    if (wave == 0) {
      v4u rf[NW];
#pragma unroll
      for (int p = 0; p < NW; ++p) rf[p] = trfrag(p);
      f32x4 acc2 = Z4;
#pragma unroll
      for (int p = 0; p < NW; ++p) acc2 = mfma32(rf[p], Fc[p], acc2);
      st_sc1(rx, pubo, nosent(acc2));
    }
    STAMPV(h * 4 + q, s, wave, 2);
    CP(5);
    tanch = now32() - (wave == 0 ? 200u : 0u);
    const v4u rpk = trfrag(wave);
    if (s < NSEG - 1) __builtin_amdgcn_s_sleep(8);
    st_sc1(rd, act ? rlane + (unsigned)s * (NH * KS * 512u) : OOB, nosentu(rpk));
  }
  CP_DUMP(h * 4 + q);
  if (budget <= 0) *(GAS unsigned*)(wb + OFF_CTRL + CT_ABORT) = 1u;
  STAMPDUMP(h * 4 + q);
}

DEV void role_O(unsigned char* wb, int rg, int cg, unsigned char* lds, int tid) {
  TUNE_LOAD
  const int lane = tid & 63, wave = rfl(tid >> 6);
  const int rho = lane & 15, g = lane >> 4;
  const rsrc_t rx = mk_rsrc(wb + OFF_XCH, (unsigned)XCH_BYTES), rd = mk_rsrc(wb + OFF_DYN, DYN_BYTES), rc = mk_rsrc(wb + OFF_CTRL, CTRL_BYTES);
  int budget = SPIN_BUDGET;
  const rsrc_t rw = mk_rsrc(wb, (unsigned)OFF_DYN);
  for (int i = 0; i < ODELAY; ++i) __builtin_amdgcn_s_sleep(16);
  v8u W[2][7]; v4u U[2], Un[2];
#pragma unroll
  for (int hh = 0; hh < 2; ++hh)
#pragma unroll
    for (int k = 0; k < 7; ++k) {
      const unsigned o = (unsigned)OFF_BTFR + (unsigned)(((2 * wave + hh) * CT + cg) * KS + 2 * k) * 1024u;
      W[hh][k] = cat8(ld_s(rw, lane * 16u, o), ld_s(rw, lane * 16u, o + 1024u));
    }
  __builtin_amdgcn_sched_barrier(0);
  const unsigned par = read_parity(wb, lane);
  const unsigned ubase = DY_UT + (unsigned)((2 * wave) * CT + cg) * 1024u;
  const unsigned poff = lane < 2 ? (unsigned)(CT_PROG + (56 + (((2 * wave + lane) * CT + cg) >> 3)) * 128) : OOB;
  int known = 0;
  if (wave == 0) STAMPW(64 + cg * 2 + rg, 11);
  U[0] = poll1(rd, ubase + lane * 16u, budget); U[1] = poll1(rd, ubase + CT * 1024 + lane * 16u, budget);
  if (wave == 0) STAMPW(64 + cg * 2 + rg, 12);
  unsigned char* mimg = lds;
  float* red = (float*)(lds + 8192);
  const unsigned aoff = (unsigned)(g * 256 + ((rho >> 2) + 4 * (rho & 1)) * 32 + ((rho >> 1) & 1) * 16);
  const int sidx = (rho & 2) ? 0xEEEE : 0x4444;
  const unsigned soff = simg_off(tid);
  const int u = rho >> 3, hh_l = (rho >> 2) & 1, ii = rho & 3;
  const int hl = 2 * wave + hh_l;
  const unsigned off_mm = (unsigned)(g * 128 + ii * 32 + u * 16);
  const unsigned off_mx = (unsigned)(512 + u * 256 + ii * 64 + g * 16);
  const int wpos = (g + 4 * ii) * 20 + hl + (u ? 1280 : 0);
  const int rpos = (((lane & 15) >> 2) + 4 * (lane >> 4) + 16 * (lane & 3)) * 20;
  const unsigned publ = (unsigned)((cg * 2 + rg) * 128 + lane * 2);
  const unsigned stepo = (wave == 7 && lane == 0 && cg == 0 && rg == 0) ? (unsigned)CT_STEP : OOB;

  const bool vact = rg == 0 && !(g & 1);
  const unsigned vlane = DY_VC + (unsigned)((wave >> 1) * CT + cg) * 1024u + (unsigned)(((2 * wave + (g >> 1)) & 3) * 16 + rho) * 16u;
  v4u vprev = {0u, 0u, 0u, 0u};
  const int nstep = rg == 0 ? NSEG : NSEG - 1;
  CP_DECL
  unsigned tanch = now32() - 100000u;
  for (int s = 0; s < nstep; ++s) {
    {
      wait_abs(tanch + TA2);
      if (wave < 7) {
        const v4u t = poll1(rx, memx_off(par, s) + tid * 16, budget);
        simg_write(mimg, soff, t);
      }
    }
    BARRIER();
    tanch = now32();
    STAMPV(64 + cg * 2 + rg, s, wave, 0);
    {
      const int sn = min(s + 1, NSEG - 2);
      if (__builtin_expect(known < sn + 1, 0)) known = wait_words(rc, poff, sn + 1, budget, false);
      Un[0] = ld_sc1s(rd, lane * 16u, ubase + (unsigned)sn * SEGB); Un[1] = ld_sc1s(rd, lane * 16u, ubase + (unsigned)sn * SEGB + CT * 1024);
      st32_sc1(rc, stepo, (unsigned)s);
      st_sc1(rd, (vact && s > 0) ? vlane + (unsigned)(s - 1) * (4u * CT * 1024u) : OOB, vprev);
    }
    __builtin_amdgcn_sched_barrier(0);
    f32x4 av0 = Z4, av1 = Z4;
    v4u af[7];
#pragma unroll
    for (int k = 0; k < 4; ++k) af[k] = *(const v4u*)(mimg + k * 1024 + aoff);
    __builtin_amdgcn_sched_barrier(0);
#pragma unroll
    for (int k = 0; k < 7; ++k) {
      if (k + 4 < 7) af[k + 4] = *(const v4u*)(mimg + (k + 4) * 1024 + aoff);
      av0 = smfma64(af[k], W[0][k], av0, sidx); av1 = smfma64(af[k], W[1][k], av1, sidx);
      __builtin_amdgcn_sched_barrier(0);
    }
    const v4u vf0 = {pk2(av0[0] + av0[2], av0[1] + av0[3]), 0u, 0u, 0u}, vf1 = {pk2(av1[0] + av1[2], av1[1] + av1[3]), 0u, 0u, 0u};
    {
      auto r = __builtin_amdgcn_permlane32_swap(vf0.x, vf1.x, false, false);
      auto a = __builtin_amdgcn_permlane16_swap(r[0], r[0], false, false);
      auto b = __builtin_amdgcn_permlane16_swap(r[1], r[1], false, false);
      vprev = nosentu((v4u){a[0], a[1], b[0], b[1]});
    }
    if (s == NSEG - 1) { st_sc1(rd, vact ? vlane + (unsigned)s * (4u * CT * 1024u) : OOB, vprev); break; }
    const unsigned sb = scx_off(par, s, rg, hl);
    const unsigned pubo = (lane & 1) ? OOB : memx_off(par, s + 1) + publ;
    const unsigned om = sb + off_mm, ox = sb + off_mx;
    const unsigned tsc = tanch + TA1;
    wait_abs(tsc - TDX);
    v4u tx = ld_sc1(rx, ox);
    wait_abs(tsc);
    STAMPV(64 + cg * 2 + rg, s, wave, 1);
    const int b1 = budget;
    CP(0);
    v4u tm = ld_sc1(rx, om);
    __builtin_amdgcn_sched_barrier(0);
    if (__builtin_expect(any_invalid(tx), 0)) tx = poll1(rx, ox, budget);
    const float x0 = f_as(tx.x), x1 = f_as(tx.y), x2 = f_as(tx.z), x3 = f_as(tx.w);
    float mx = vmax(vmax(x0, x1), vmax(x2, x3));
    mx = vmax(mx, dppf<DPP_ROR8>(mx));
    mx = xg_max(mx);
    const float e0 = __builtin_amdgcn_exp2f(x0 - mx), e1 = __builtin_amdgcn_exp2f(x1 - mx), e2 = __builtin_amdgcn_exp2f(x2 - mx), e3 = __builtin_amdgcn_exp2f(x3 - mx);
    const unsigned q01 = pk2(e0, e1), q23 = pk2(e2, e3);
    const v4u bx = {q01, q23, dppu<DPP_ROR8>(q01), dppu<DPP_ROR8>(q23)};
    f32x4 o0 = mfma32(U[0], bx, Z4), o1 = mfma32(U[1], bx, Z4);
    float lx = (e0 + e1) + (e2 + e3);
    lx += dppf<DPP_ROR8>(lx);
    lx = xg_sum(lx);
    asm volatile("" : "+v"(o0), "+v"(o1), "+v"(lx), "+v"(mx));
    __builtin_amdgcn_sched_barrier(0);
    if (__builtin_expect(any_invalid(tm), 0)) tm = poll1(rx, om, budget);
    if (wave) STAMPX(s, wave, 3, 1 + b1 - budget);
    STAMPV(64 + cg * 2 + rg, s, wave, 2);
    CP(1);
    auto p0 = __builtin_amdgcn_permlane32_swap(tm.x, tm.z, false, false);
    auto p1 = __builtin_amdgcn_permlane32_swap(tm.y, tm.w, false, false);
    float a0 = f_as(p0[0]) + f_as(p0[1]), a1 = f_as(p1[0]) + f_as(p1[1]);
    auto p2 = __builtin_amdgcn_permlane16_swap(u_as(a0), u_as(a1), false, false);
    const float sm = f_as(p2[0]) + f_as(p2[1]);
    float m = vmax(vmax(sm, dppf<DPP_ROR8>(sm)), mx);
    m = xg_max(m);
    CPV(m); CP(2);
    const float em = __builtin_amdgcn_exp2f(sm - m), fx = __builtin_amdgcn_exp2f(mx - m);
    const float em2 = dppf<DPP_ROR8>(em);
    const v4u bm = {pk2(em, em2), 0u, 0u, 0u};
    CPV(bm); CP(3);
    o0 = mfma32(vf0, bm, o0 * fx); o1 = mfma32(vf1, bm, o1 * fx);
    const float l = xg_sum(em + em2) + fx * lx;
    const float il = __builtin_amdgcn_rcpf(l);
#pragma unroll
    for (int r = 0; r < 4; ++r) red[wpos + 320 * r] = (hh_l ? o1[r] : o0[r]) * il;
    CP(4);
    BARRIER();
    CP(5);
    tanch = now32();
    if (wave == 0) {
      f32x4 a = *(const f32x4*)(red + rpos), b = *(const f32x4*)(red + rpos + 4), c = *(const f32x4*)(red + rpos + 8), d = *(const f32x4*)(red + rpos + 12);
      f32x4 r4 = (a + b) + (c + d);
      float v = (r4[0] + r4[1]) + (r4[2] + r4[3]);
      st32_sc1(rx, pubo, min(pk2(v, dppf<DPP_XOR1>(v)), 0xFFFFFFFEu));
    }
    if (wave == 0) STAMPV(64 + cg * 2 + rg, s, wave, 3);
    CP(6);
    U[0] = Un[0]; U[1] = Un[1];
  }
  CP_DUMP(64 + cg * 2 + rg);
  if (budget <= 0) *(GAS unsigned*)(wb + OFF_CTRL + CT_ABORT) = 1u;
  STAMPDUMP(64 + cg * 2 + rg);
}

DEV float row16_max(float v) {
  v = vmax(v, dppf<DPP_XOR1>(v)); v = vmax(v, dppf<DPP_XOR2>(v));
  v = vmax(v, dppf<DPP_ROR4>(v)); v = vmax(v, dppf<DPP_ROR8>(v)); return v;
}
DEV float row16_sum(float v) {
  v += dppf<DPP_XOR1>(v); v += dppf<DPP_XOR2>(v); v += dppf<DPP_ROR4>(v); v += dppf<DPP_ROR8>(v); return v;
}
DEV int ticket(unsigned char* wb, int ctr, unsigned char* lds, int tid) {
  volatile LAS int* sl = (volatile LAS int*)(lds + 114688 - 16);
  __syncthreads();
  if (tid == 0) sl[0] = (int)__hip_atomic_fetch_add((GAS unsigned*)(wb + OFF_CTRL + ctr), 1u, __ATOMIC_RELAXED, __HIP_MEMORY_SCOPE_AGENT);
  __syncthreads();
  return rfl(sl[0]);
}
DEV void parta_unit1(const In& in, unsigned char* wb, int u, unsigned char* lds, int tid) {
  const int lane = tid & 63, wave = rfl(tid >> 6);
  const int rho = lane & 15, g = lane >> 4;
  const int h = u & 15, part = u >> 4;
  const rsrc_t rd = mk_rsrc(wb + OFF_DYN, DYN_BYTES), rw = mk_rsrc(wb, (unsigned)OFF_DYN);
#pragma unroll
  for (int i = 0; i < 7; ++i) {
    const int f = i * 8 + wave;
    const unsigned src = f < 28 ? (unsigned)OFF_WKF + h * 28672u + f * 1024u : (unsigned)OFF_WVF + h * 28672u + (f - 28) * 1024u;
    lds_dma(rw, lds + f * 1024, lane * 16u, src);
  }
  const int s = part * 8 + wave;
  const u16* xp0 = in.Ax + (size_t)(s * 32 + rho) * DM + g * 8;
  const u16* xp1 = xp0 + 16 * DM;
  v4u X0[KS], X1[KS];
#pragma unroll
  for (int k = 0; k < KS; ++k) { X0[k] = *(const v4u*)(xp0 + k * 32); X1[k] = *(const v4u*)(xp1 + k * 32); }
  DRAIN_BARRIER();
  f32x4 pk_[2][2], vv[2][2];
#pragma unroll
  for (int t = 0; t < 2; ++t)
#pragma unroll
    for (int b = 0; b < 2; ++b) { pk_[t][b] = Z4; vv[t][b] = Z4; }
#pragma unroll
  for (int k = 0; k < KS; ++k) {
    const v4u k0 = *(const v4u*)(lds + k * 1024 + lane * 16), k1 = *(const v4u*)(lds + (KS + k) * 1024 + lane * 16);
    const v4u v0 = *(const v4u*)(lds + 28672 + k * 1024 + lane * 16), v1 = *(const v4u*)(lds + 28672 + (KS + k) * 1024 + lane * 16);
    pk_[0][0] = mfma32(X0[k], k0, pk_[0][0]); pk_[0][1] = mfma32(X0[k], k1, pk_[0][1]);
    pk_[1][0] = mfma32(X1[k], k0, pk_[1][0]); pk_[1][1] = mfma32(X1[k], k1, pk_[1][1]);
    vv[0][0] = mfma32(X0[k], v0, vv[0][0]); vv[0][1] = mfma32(X0[k], v1, vv[0][1]);
    vv[1][0] = mfma32(X1[k], v0, vv[1][0]); vv[1][1] = mfma32(X1[k], v1, vv[1][1]);
  }
  const bool pad = rho >= 12;
  f32x4 pp[2][2], pn[2][2];
#pragma unroll
  for (int t = 0; t < 2; ++t)
#pragma unroll
    for (int r = 0; r < 4; ++r) {
      const float a0 = pk_[t][0][r], a1 = pad ? a0 : pk_[t][1][r];
      const float mx = row16_max(vmax(a0, a1));
      const float mn = -row16_max(vmax(-a0, -a1));
      const float ep0 = __builtin_amdgcn_exp2f(a0 - mx), ep1 = pad ? 0.f : __builtin_amdgcn_exp2f(a1 - mx);
      const float en0 = __builtin_amdgcn_exp2f(mn - a0), en1 = pad ? 0.f : __builtin_amdgcn_exp2f(mn - a1);
      const float ip = __builtin_amdgcn_rcpf(row16_sum(ep0 + ep1)), in_ = __builtin_amdgcn_rcpf(row16_sum(en0 + en1));
      pp[t][0][r] = ep0 * ip; pp[t][1][r] = ep1 * ip; pn[t][0][r] = en0 * in_; pn[t][1][r] = en1 * in_;
    }
  if (rho == 12) { vv[0][1] = (f32x4){1.f, 1.f, 1.f, 1.f}; vv[1][1] = (f32x4){1.f, 1.f, 1.f, 1.f}; }
  const v4u vb0 = pack8(vv[0][0], vv[1][0]), vb1 = pack8(vv[0][1], vv[1][1]);
  v4u fa[4] = {pack8(pp[0][0], pp[1][0]), pack8(pp[0][1], pp[1][1]), pack8(pn[0][0], pn[1][0]), pack8(pn[0][1], pn[1][1])};
  BARRIER();
  float* red = (float*)lds;
#pragma unroll
  for (int a = 0; a < 4; ++a) {
    *(f32x4*)(red + ((wave * 8 + a * 2 + 0) * 64 + lane) * 4) = mfma32(fa[a], vb0, Z4);
    *(f32x4*)(red + ((wave * 8 + a * 2 + 1) * 64 + lane) * 4) = mfma32(fa[a], vb1, Z4);
  }
  BARRIER();
  {
    f32x4 sum = Z4;
#pragma unroll
    for (int w = 0; w < 8; ++w) sum += *(const f32x4*)(red + (w * 512 + tid) * 4);
    st_sc1(rd, DY_KVP + (unsigned)(part * NH + h) * 8192u + tid * 16u, __builtin_bit_cast(v4u, sum));
  }
  DRAIN_BARRIER();
  const int arrived = ticket(wb, CT_HCNT + h * 128, lds, tid);
  if (arrived == 7) {
    if (wave < 4) {
      const int sg = wave >> 1, b = wave & 1;
      v4u lo[8], hi[8];
#pragma unroll
      for (int p = 0; p < 8; ++p) {
        lo[p] = ld_sc1s(rd, lane * 16u, DY_KVP + (unsigned)(p * NH + h) * 8192u + (unsigned)((sg * 2 + 0) * 2 + b) * 1024u);
        hi[p] = ld_sc1s(rd, lane * 16u, DY_KVP + (unsigned)(p * NH + h) * 8192u + (unsigned)((sg * 2 + 1) * 2 + b) * 1024u);
      }
      f32x4 l4 = Z4, h4 = Z4;
#pragma unroll
      for (int p = 0; p < 8; ++p) { l4 += __builtin_bit_cast(f32x4, lo[p]); h4 += __builtin_bit_cast(f32x4, hi[p]); }
      st_sc1(rd, DY_KV + (unsigned)(h * 4 + wave) * 1024u + lane * 16u, pack8(l4, h4));
    }
    DRAIN_BARRIER();
    if (tid == 0) __hip_atomic_fetch_add((GAS unsigned*)(wb + OFF_CTRL + CT_ACNT), 1u, __ATOMIC_RELAXED, __HIP_MEMORY_SCOPE_AGENT);
  }
}

DEV void parta_unit2(const In& in, unsigned char* wb, int u, unsigned char* lds, int tid) {
  const int lane = tid & 63, wave = rfl(tid >> 6);
  const int rho = lane & 15, g = lane >> 4;
  const rsrc_t rd = mk_rsrc(wb + OFF_DYN, DYN_BYTES), rc = mk_rsrc(wb + OFF_CTRL, CTRL_BYTES);
  const rsrc_t rw = mk_rsrc(wb, (unsigned)OFF_DYN);
  int budget = SPIN_BUDGET;
#pragma unroll
  for (int i = 0; i < 4; ++i) {
    const int f = i * 8 + wave;
    if (f < 28) {
      const int tt = f / KS, ks = f % KS;
      __builtin_amdgcn_global_load_lds((const unsigned*)(in.Ax + (size_t)(u * 32 + tt * 16 + rho) * DM + ks * 32 + g * 8),
                                       (LAS unsigned*)(lds + f * 1024), 16, 0, 0);
    }
  }
  unsigned char* ob_l = lds + 28672;
  f32x4 p[2][2][2];
#pragma unroll
  for (int hh = 0; hh < 2; ++hh) {
    const unsigned wq = (unsigned)OFF_WQF + (unsigned)(2 * wave + hh) * 28672u;
    v4u Wf[2][KS];
    asm volatile("" ::: "memory");
#pragma unroll
    for (int k = 0; k < KS; ++k) { Wf[0][k] = ld_s(rw, lane * 16u, wq + k * 1024); Wf[1][k] = ld_s(rw, lane * 16u, wq + (KS + k) * 1024); }
    if (hh == 0) DRAIN_BARRIER();
    p[hh][0][0] = Z4; p[hh][0][1] = Z4; p[hh][1][0] = Z4; p[hh][1][1] = Z4;
#pragma unroll
    for (int k = 0; k < KS; ++k) {
      const v4u x0 = *(const v4u*)(lds + k * 1024 + lane * 16), x1 = *(const v4u*)(lds + (KS + k) * 1024 + lane * 16);
      p[hh][0][0] = mfma32(Wf[0][k], x0, p[hh][0][0]); p[hh][1][0] = mfma32(Wf[1][k], x0, p[hh][1][0]);
      p[hh][0][1] = mfma32(Wf[0][k], x1, p[hh][0][1]); p[hh][1][1] = mfma32(Wf[1][k], x1, p[hh][1][1]);
    }
    PIN2(p[hh][0][0], p[hh][0][1]); PIN2(p[hh][1][0], p[hh][1][1]);
  }
  wait_words(rc, lane == 0 ? (unsigned)CT_ACNT : OOB, NH, budget, true);
  const bool pad = g == 3;
#pragma unroll
  for (int hh = 0; hh < 2; ++hh) {
    const int h = 2 * wave + hh;
    v4u kvf[4];
#pragma unroll
    for (int a = 0; a < 4; ++a) kvf[a] = ld_sc1s(rd, lane * 16u, DY_KV + (unsigned)(h * 4 + a) * 1024u);
#pragma unroll
    for (int tt = 0; tt < 2; ++tt) {
      const f32x4 p0 = p[hh][0][tt], p1 = pad ? p0 : p[hh][1][tt];
      float mx = vmax(vmax(vmax(p0[0], p0[1]), vmax(p0[2], p0[3])), vmax(vmax(p1[0], p1[1]), vmax(p1[2], p1[3])));
      float mn = vmax(vmax(vmax(-p0[0], -p0[1]), vmax(-p0[2], -p0[3])), vmax(vmax(-p1[0], -p1[1]), vmax(-p1[2], -p1[3])));
      mx = xg_max(mx); mn = -xg_max(mn);
      f32x4 ep0, ep1, en0, en1;
      float sp = 0.f, sn = 0.f;
#pragma unroll
      for (int r = 0; r < 4; ++r) {
        ep0[r] = __builtin_amdgcn_exp2f(p0[r] - mx); ep1[r] = pad ? 0.f : __builtin_amdgcn_exp2f(p1[r] - mx);
        en0[r] = __builtin_amdgcn_exp2f(mn - p0[r]); en1[r] = pad ? 0.f : __builtin_amdgcn_exp2f(mn - p1[r]);
        sp += ep0[r] + ep1[r]; sn += en0[r] + en1[r];
      }
      const float ip = __builtin_amdgcn_rcpf(xg_sum(sp)), in_ = __builtin_amdgcn_rcpf(xg_sum(sn));
      const v4u bp = pack8(ep0 * ip, ep1 * ip), bn = pack8(en0 * in_, en1 * in_);
      f32x4 n0 = mfma32(kvf[0], bp, Z4), n1 = mfma32(kvf[1], bp, Z4);
      n0 = mfma32(kvf[2], bn, n0); n1 = mfma32(kvf[3], bn, n1);
      const float den = xg_sum(pad ? n1[0] : 0.f) + 1e-6f;
      const float id = 1.f / den;
      if (pad) n1 = Z4;
      *(v4u*)(ob_l + (h * 2 + tt) * 1024 + lane * 16) = pack8(n0 * id, n1 * id);
    }
  }
  const int wv = wave < 7 ? wave : 6;
  const unsigned wo = (unsigned)OFF_WOF + (unsigned)(wv * 4) * 1024u;
  f32x4 acc[4][2];
#pragma unroll
  for (int n = 0; n < 4; ++n) { acc[n][0] = Z4; acc[n][1] = Z4; }
#pragma unroll
  for (int hb = 0; hb < 2; ++hb) {
    v4u Wo_[8][4];
    asm volatile("" ::: "memory");
#pragma unroll
    for (int h8 = 0; h8 < 8; ++h8)
#pragma unroll
      for (int n = 0; n < 4; ++n) Wo_[h8][n] = ld_s(rw, lane * 16u, wo + (unsigned)((hb * 8 + h8) * CT + n) * 1024u);
    if (hb == 0) BARRIER();
#pragma unroll
    for (int h8 = 0; h8 < 8; ++h8) {
      const int h = hb * 8 + h8;
      const v4u o0 = *(const v4u*)(ob_l + (h * 2) * 1024 + lane * 16), o1 = *(const v4u*)(ob_l + (h * 2 + 1) * 1024 + lane * 16);
#pragma unroll
      for (int n = 0; n < 4; ++n) { acc[n][0] = mfma32(Wo_[h8][n], o0, acc[n][0]); acc[n][1] = mfma32(Wo_[h8][n], o1, acc[n][1]); }
    }
#pragma unroll
    for (int n = 0; n < 4; ++n) PIN2(acc[n][0], acc[n][1]);
  }
  if (wave < 7) {
#pragma unroll
    for (int tt = 0; tt < 2; ++tt)
#pragma unroll
      for (int np = 0; np < 2; ++np)
        *(v4u*)(in.out + (size_t)(u * 32 + tt * 16 + rho) * 896 + (wave * 2 + np) * 32 + 8 * g) = pack8(acc[2 * np][tt], acc[2 * np + 1][tt]);
  }
}

DEV void parta_all(const In& in, unsigned char* wb, unsigned char* lds, int tid) {
  for (;;) { const int u = ticket(wb, CT_Q1, lds, tid); if (u >= 128) break; parta_unit1(in, wb, u, lds, tid); }
  for (;;) { const int u = ticket(wb, CT_Q2, lds, tid); if (u >= 64) break; parta_unit2(in, wb, u, lds, tid); }
}

DEV void unit_XS(unsigned char* wb, int s, int hq, unsigned char* lds, int tid) {
  const int lane = tid & 63, wave = rfl(tid >> 6);
  const int rho = lane & 15, g = lane >> 4;
  const int h = hq * 4 + (wave >> 1), tt = wave & 1;
  const rsrc_t rw = mk_rsrc(wb, (unsigned)OFF_DYN);
  const rsrc_t rd = mk_rsrc(wb + OFF_DYN, DYN_BYTES), rc = mk_rsrc(wb + OFF_CTRL, CTRL_BYTES);
  int budget = SPIN_BUDGET;
  const int tok = sig(tt, rho);
  v4u X[KS];
#pragma unroll
  for (int k = 0; k < KS; ++k) X[k] = ld_s(rw, lane * 16u, (unsigned)OFF_XFR + (unsigned)((s * 2 + tt) * KS + k) * 1024u);
  if (wave == 0) wait_words(rc, lane < 14 ? (unsigned)(CT_PROG + (hq * 14 + lane) * 128) : OOB, s + 1, budget, true);
  BARRIER();
  const unsigned gp = DY_GT + (unsigned)s * SEGB + (unsigned)(h * 2 * KS) * 1024u;
  f32x4 sx0 = Z4, sx1 = Z4, sm = Z4;
  {
    v4u G0[KS], G1[KS];
#pragma unroll
    for (int k = 0; k < KS; ++k) { G0[k] = ld_sc1s(rd, lane * 16u, gp + k * 1024); G1[k] = ld_sc1s(rd, lane * 16u, gp + (KS + k) * 1024); }
#pragma unroll
    for (int k = 0; k < KS; ++k) { sx0 = mfma32(G0[k], X[k], sx0); sx1 = mfma32(G1[k], X[k], sx1); }
    PIN2(sx0, sx1);
  }
  if (wave == 0) {
    wait_words(rc, lane == 0 ? (unsigned)CT_STEP : OOB, s < NSEG - 2 ? s + 2 : NSEG - 2, budget, true);
  }
  BARRIER();
  {
    const int key = ((rho & 7) >> 1) + 4 * (rho & 1);
    const unsigned rl = (unsigned)(g * 8 + key) * 16u;
    if (s >= NSEG - 2) poll1(rd, lane < KS ? DY_RC + (unsigned)((s * NH + h) * KS + lane) * 512u : OOB, budget);
    v4u R[KS];
    bool bad;
#pragma nounroll
    do {
      asm volatile("" ::: "memory");
      unsigned mm = 0;
#pragma unroll
      for (int k = 0; k < KS; ++k) R[k] = ld_sc1s(rd, rl, DY_RC + (unsigned)((s * NH + h) * KS + k) * 512u);
#pragma unroll
      for (int k = 0; k < KS; ++k) mm = max(mm, max(max(R[k].x, R[k].y), max(R[k].z, R[k].w)));
      bad = __any(mm == 0xFFFFFFFFu);
      budget -= 4;
    } while (bad && budget > 0);
#pragma unroll
    for (int k = 0; k < KS; ++k) sm = mfma32(R[k], X[k], sm);
    if (s == 63) STAMPG(hq, 14);
  }
  float v[8], w[4];
  float m = -3.0e38f;
#pragma unroll
  for (int j = 0; j < 8; ++j) { v[j] = (8 * g + j <= tok) ? (j < 4 ? sx0[j & 3] : sx1[j & 3]) : -3.0e38f; m = vmax(m, v[j]); }
#pragma unroll
  for (int j = 0; j < 4; ++j) { w[j] = g < 2 ? sm[j] : -3.0e38f; m = vmax(m, w[j]); }
  m = xg_max(m);
  float l = 0.f;
#pragma unroll
  for (int j = 0; j < 8; ++j) { v[j] = (8 * g + j <= tok) ? __builtin_amdgcn_exp2f(v[j] - m) : 0.f; l += v[j]; }
#pragma unroll
  for (int j = 0; j < 4; ++j) { w[j] = g < 2 ? __builtin_amdgcn_exp2f(w[j] - m) : 0.f; l += w[j]; }
  l = xg_sum(l);
  const float il = __builtin_amdgcn_rcpf(l);
  const v4u px = {pk2(v[0] * il, v[1] * il), pk2(v[2] * il, v[3] * il), pk2(v[4] * il, v[5] * il), pk2(v[6] * il, v[7] * il)};
  const v2u pm = {min(pk2(w[0] * il, w[1] * il), 0xFFFFFFFEu), min(pk2(w[2] * il, w[3] * il), 0xFFFFFFFEu)};
  st_sc1(rd, DY_PX + (unsigned)((s * NH + h) * 2 + tt) * 1024u + lane * 16u, nosentu(px));
  __builtin_amdgcn_raw_buffer_store_b64(pm, rd, g < 2 ? DY_PM + (unsigned)((s * 4 + hq) * 2 + tt) * 1024u + (unsigned)((h & 3) * 16 + rho) * 16u + 8u * g : OOB, 0, 16);
  if (s == 63) STAMPG(hq, 15);
  DRAIN_BARRIER();
  if (tid == 0) __hip_atomic_fetch_add((GAS unsigned*)(wb + OFF_CTRL + CT_XCNT + s * 128), 1u, __ATOMIC_RELAXED, __HIP_MEMORY_SCOPE_AGENT);
}

DEV void unit_XO(const In& in, unsigned char* wb, int s, int qt, unsigned char* lds, int tid) {
  const int lane = tid & 63, wave = rfl(tid >> 6);
  const int rho = lane & 15, g = lane >> 4;
  const rsrc_t rd = mk_rsrc(wb + OFF_DYN, DYN_BYTES), rc = mk_rsrc(wb + OFF_CTRL, CTRL_BYTES);
  int budget = SPIN_BUDGET;
  const int ct = qt * 7 + (wave < 7 ? wave : 6);
  if (wave == 0) wait_words(rc, lane < 56 ? (unsigned)(CT_PROG + (56 + lane) * 128) : OOB, s + 1, budget, true);
  BARRIER();
  v4u U[NH];
#pragma unroll
  for (int h = 0; h < NH; ++h) U[h] = ld_sc1s(rd, lane * 16u, DY_UT + (unsigned)s * SEGB + (unsigned)(h * CT + ct) * 1024u);
  if (wave == 0) {
    wait_words(rc, lane == 0 ? (unsigned)CT_STEP : OOB, s < NSEG - 2 ? s + 2 : NSEG - 2, budget, true);
  }
  BARRIER();
  v4u V[4];
  {
    bool bad;
#pragma nounroll
    do {
      asm volatile("" ::: "memory");
      unsigned mm = 0;
#pragma unroll
      for (int hq = 0; hq < 4; ++hq) V[hq] = ld_sc1s(rd, lane * 16u, DY_VC + (unsigned)((s * 4 + hq) * CT + ct) * 1024u);
#pragma unroll
      for (int hq = 0; hq < 4; ++hq) mm = max(mm, max(max(V[hq].x, V[hq].y), max(V[hq].z, V[hq].w)));
      bad = __any(mm == 0xFFFFFFFFu);
      budget -= 2;
    } while (bad && budget > 0);
  }
  if (s == 63) STAMPG(qt, 16);
  if (wave == 0 && s < NSEG - 2) wait_words(rc, lane == 0 ? (unsigned)(CT_XCNT + s * 128) : OOB, 4, budget, true);
  BARRIER();
  {
    if (s >= NSEG - 2) poll1(rd, lane < 32 ? DY_PX + (unsigned)s * 32768u + lane * 1024u : OOB, budget);
    v4u t[5];
    bool bad;
#pragma nounroll
    do {
      asm volatile("" ::: "memory");
      unsigned mm = 0;
#pragma unroll
      for (int i = 0; i < 4; ++i) t[i] = ld_sc1s(rd, tid * 16u, DY_PX + (unsigned)s * 32768u + i * 8192u);
      t[4] = ld_sc1s(rd, tid * 16u, DY_PM + (unsigned)s * 8192u);
#pragma unroll
      for (int i = 0; i < 5; ++i) mm = max(mm, max(max(t[i].x, t[i].y), max(t[i].z, t[i].w)));
      bad = __any(mm == 0xFFFFFFFFu);
      budget -= 2;
    } while (bad && budget > 0);
#pragma unroll
    for (int i = 0; i < 5; ++i) *(v4u*)(lds + (i * 512 + tid) * 16) = t[i];
  }
  BARRIER();
  if (s == 63) STAMPG(qt, 17);
  f32x4 a0 = Z4, a1 = Z4;
#pragma unroll
  for (int h = 0; h < NH; ++h) {
    a0 = mfma32(U[h], *(const v4u*)(lds + (h * 2) * 1024 + lane * 16), a0);
    a1 = mfma32(U[h], *(const v4u*)(lds + (h * 2 + 1) * 1024 + lane * 16), a1);
  }
#pragma unroll
  for (int hq = 0; hq < 4; ++hq) {
    a0 = mfma32(V[hq], *(const v4u*)(lds + 32768 + (hq * 2) * 1024 + lane * 16), a0);
    a1 = mfma32(V[hq], *(const v4u*)(lds + 32768 + (hq * 2 + 1) * 1024 + lane * 16), a1);
  }
  if (wave < 7) {
    v2u o0 = {pk2(a0[0], a0[1]), pk2(a0[2], a0[3])}, o1 = {pk2(a1[0], a1[1]), pk2(a1[2], a1[3])};
    *(v2u*)(in.out + (size_t)(s * 32 + sig(0, rho)) * 896 + 448 + ct * 16 + 4 * g) = o0;
    *(v2u*)(in.out + (size_t)(s * 32 + sig(1, rho)) * 896 + 448 + ct * 16 + 4 * g) = o1;
  }
}

DEV int get_slot(unsigned char* wb, unsigned char* lds, int tid) {
  int* sl = (int*)lds;
  if (tid == 0) {
    GAS unsigned* tick = (GAS unsigned*)(wb + OFF_CTRL + CT_TICK);
    const unsigned x = xcc_id() & 7u;
    const unsigned r = __hip_atomic_fetch_add(tick + x * 64, 1u, __ATOMIC_RELAXED, __HIP_MEMORY_SCOPE_AGENT);
    int slot;
    if (r < 32) slot = (int)(x * 32 + r);
    else {
      unsigned c[8]; int spins = 0;
      for (;;) {
        unsigned tot = 0;
#pragma unroll
        for (int k = 0; k < 8; ++k) { c[k] = __hip_atomic_load(tick + k * 64, __ATOMIC_RELAXED, __HIP_MEMORY_SCOPE_AGENT); tot += c[k]; }
        if (tot >= 256u || ++spins > SPIN_BUDGET) break;
      }
      unsigned idx = r - 32;
#pragma unroll
      for (int k = 0; k < 8; ++k) idx += ((unsigned)k < x && c[k] > 32) ? c[k] - 32 : 0;
      slot = -1;
#pragma unroll
      for (int k = 0; k < 8; ++k) {
        const unsigned fr = c[k] < 32 ? 32 - c[k] : 0;
        if (slot < 0) { if (idx < fr) slot = (int)(k * 32 + c[k] + idx); else idx -= fr; }
      }
    }
    sl[0] = slot;
  }
  __syncthreads();
  const int slot = rfl(sl[0]);
  __syncthreads();
  return slot;
}

constexpr int MAIN_LDS = 114688;
__global__ void __launch_bounds__(512, 2) k_main(In in) {
  extern __shared__ __attribute__((aligned(16))) unsigned char lds[];
  unsigned char* wb = ws_base(in.ws);
  const int tid = threadIdx.x;
  const int slot = get_slot(wb, lds, tid);
  if (slot < 0) return;
  STAMPG(slot, 0);
  const int idx = (slot & 31) * 4 + ((slot >> 5) & 3);
  if (slot < 128) {
    if (idx < 64) {
      const int h = idx >> 2, q = idx & 3;
      if (q < 2) role_S<true>(wb, h, q, lds, tid); else role_S<false>(wb, h, q, lds, tid);
    } else if (idx < 120) {
      const int o = idx - 64;
      role_O(wb, (o >> 2) & 1, (o >> 3) * 4 + (o & 3), lds, tid);
    }
  } else {
    if (idx < 56) gu_worker<true>(wb, idx, lds, tid);
    else if (idx < 112) gu_worker<false>(wb, idx, lds, tid);
    else for (int i = 0; i < MDELAY; ++i) __builtin_amdgcn_s_sleep(16);
    parta_all(in, wb, lds, tid);
    STAMPG(slot, 2);
    for (;;) {
      const int u = ticket(wb, CT_Q3, lds, tid);
      if (u >= NSEG * 8) break;
      if (u & 4) unit_XO(in, wb, u >> 3, u & 3, lds, tid); else unit_XS(wb, u >> 3, u & 3, lds, tid);
      if (u >= NSEG * 8 - 16) break;
    }
  }
  STAMPG(slot, 3);
}

extern "C" void kernel_launch(void* const* d_in, const int* in_sizes, int n_in,
                              void* d_out, int out_size, void* d_ws, size_t ws_size,
                              hipStream_t stream) {
  In in;
  in.Ax = (const u16*)d_in[0]; in.Awqkv = (const u16*)d_in[1]; in.Awphi = (const u16*)d_in[2]; in.Awo = (const u16*)d_in[3];
  in.Bx = (const u16*)d_in[4]; in.Bmem0 = (const u16*)d_in[5]; in.Wq = (const u16*)d_in[6]; in.Wk = (const u16*)d_in[7];
  in.Wv = (const u16*)d_in[8]; in.Wo = (const u16*)d_in[9];
  in.out = (u16*)d_out; in.ws = d_ws;
  (void)hipFuncSetAttribute((const void*)k_prep, hipFuncAttributeMaxDynamicSharedMemorySize, 57344);
  (void)hipFuncSetAttribute((const void*)k_main, hipFuncAttributeMaxDynamicSharedMemorySize, MAIN_LDS);
  k_prep<<<PREP_GRID, 448, 57344, stream>>>(in);
  k_main<<<256, 512, MAIN_LDS, stream>>>(in);
}
```

```cpp
#include <hip/hip_runtime.h>
#include <hip/hip_bf16.h>

typedef unsigned short u16;
typedef __bf16 bf16x8 __attribute__((ext_vector_type(8)));
typedef __bf16 bf16x2 __attribute__((ext_vector_type(2)));
typedef short s16x4 __attribute__((ext_vector_type(4)));
typedef float f32x4 __attribute__((ext_vector_type(4)));
typedef unsigned v4u __attribute__((ext_vector_type(4)));
typedef unsigned v2u __attribute__((ext_vector_type(2)));
typedef unsigned v8u __attribute__((ext_vector_type(8)));
typedef __bf16 bf16x16 __attribute__((ext_vector_type(16)));
#define DEV __device__ __forceinline__
#define LAS __attribute__((address_space(3)))
#define GAS __attribute__((address_space(1)))

constexpr int DM = 448, NH = 16, EH = 256, NSEG = 64, KS = 14, CT = 28;
constexpr size_t MB = 1u << 20;
constexpr size_t OFF_XCH = 0;
constexpr size_t XCH_BYTES = 6 * MB;
constexpr int XCH_PAIRS_USED = 316;
constexpr size_t OFF_CTRL = 6 * MB;
constexpr size_t OFF_AFR = 8 * MB;
constexpr size_t OFF_BTFR = 16 * MB;
constexpr size_t OFF_XFR = 24 * MB;
constexpr size_t OFF_WQF = 26 * MB;
constexpr size_t OFF_WKF = 27 * MB;
constexpr size_t OFF_WVF = 28 * MB;
constexpr size_t OFF_WOF = 29 * MB;
constexpr size_t OFF_KVF = 30 * MB;
constexpr size_t OFF_DYN = 32 * MB;
constexpr unsigned DY_GT = 0;
constexpr unsigned DY_UT = 32u << 20;
constexpr unsigned DY_RC = 64u << 20;
constexpr unsigned DY_VC = 72u << 20;
constexpr unsigned DY_PX = 80u << 20;
constexpr unsigned DY_PM = 84u << 20;
constexpr unsigned DY_KV = 86u << 20;
constexpr unsigned DY_KVP = 87u << 20;
constexpr unsigned DYN_BYTES = 88u << 20;
constexpr unsigned SEGB = NH * CT * 1024;
constexpr unsigned OOB = 0xFFFFFF00u;
constexpr int SPIN_BUDGET = 1 << 20;
constexpr int CT_VOTE = 0;
constexpr int CT_TICK = 4096;
constexpr int CT_ABORT = 8192;
constexpr int CT_STEP = 8448;
constexpr int CT_ACNT = 8704;
constexpr int CT_Q1 = 8960;
constexpr int CT_Q2 = 9216;
constexpr int CT_HCNT = 10240;
constexpr int CT_PROG = 16384;
constexpr int CT_Q3 = 9472;
constexpr int CT_DONE = 12544;
constexpr int CT_XCNT = 32768;
constexpr int CTRL_BYTES = 40960;

DEV unsigned char* ws_base(void* ws) { const size_t a = (size_t)ws; return (unsigned char*)ws + (((a + (2 * MB - 1)) & ~(size_t)(2 * MB - 1)) - a); }
DEV unsigned pk2(float a, float b) { bf16x2 v = {(__bf16)a, (__bf16)b}; return __builtin_bit_cast(unsigned, v); }
DEV f32x4 mfma32(v4u a, v4u b, f32x4 c) { return __builtin_amdgcn_mfma_f32_16x16x32_bf16(__builtin_bit_cast(bf16x8, a), __builtin_bit_cast(bf16x8, b), c, 0, 0, 0); }
DEV f32x4 smfma64(v4u a, v8u b, f32x4 c, int idx) { return __builtin_amdgcn_smfmac_f32_16x16x64_bf16(__builtin_bit_cast(bf16x8, a), __builtin_bit_cast(bf16x16, b), c, idx, 0, 0); }
DEV v8u cat8(v4u a, v4u b) { return __builtin_shufflevector(a, b, 0, 1, 2, 3, 4, 5, 6, 7); }
DEV v4u pack8(f32x4 a, f32x4 b) { v4u r; r.x = pk2(a[0], a[1]); r.y = pk2(a[2], a[3]); r.z = pk2(b[0], b[1]); r.w = pk2(b[2], b[3]); return r; }
DEV int sig(int tau, int rho) { return 8 * (rho >> 2) + 4 * tau + (rho & 3); }
DEV unsigned xcc_id() { return (unsigned)__builtin_amdgcn_s_getreg(20 | (3 << 11)) & 0xfu; }
DEV int rfl(int x) { return __builtin_amdgcn_readfirstlane(x); }
DEV float f_as(unsigned x) { return __builtin_bit_cast(float, x); }
DEV unsigned u_as(float x) { return __builtin_bit_cast(unsigned, x); }
template <int CTRL> DEV float dppf(float x) { return f_as((unsigned)__builtin_amdgcn_update_dpp(0, (int)u_as(x), CTRL, 0xf, 0xf, true)); }
template <int CTRL> DEV unsigned dppu(unsigned x) { return (unsigned)__builtin_amdgcn_update_dpp(0, (int)x, CTRL, 0xf, 0xf, true); }
constexpr int DPP_ROR8 = 0x128, DPP_ROR4 = 0x124, DPP_XOR1 = 0xB1, DPP_XOR2 = 0x4E;
DEV float vmax(float a, float b) { return __builtin_elementwise_maximum(a, b); }
DEV float xg_max(float m) {
  auto a = __builtin_amdgcn_permlane32_swap(u_as(m), u_as(m), false, false);
  m = vmax(f_as(a[0]), f_as(a[1]));
  auto b = __builtin_amdgcn_permlane16_swap(u_as(m), u_as(m), false, false);
  return vmax(f_as(b[0]), f_as(b[1]));
}
DEV float xg_sum(float m) {
  auto a = __builtin_amdgcn_permlane32_swap(u_as(m), u_as(m), false, false);
  m = f_as(a[0]) + f_as(a[1]);
  auto b = __builtin_amdgcn_permlane16_swap(u_as(m), u_as(m), false, false);
  return f_as(b[0]) + f_as(b[1]);
}
typedef __amdgpu_buffer_rsrc_t rsrc_t;
DEV rsrc_t mk_rsrc(void* p, unsigned bytes) { return __builtin_amdgcn_make_buffer_rsrc(p, 0, (int)bytes, 0x00020000); }
DEV v4u ld_sc1(rsrc_t r, unsigned off) { return __builtin_amdgcn_raw_buffer_load_b128(r, off, 0, 16); }
DEV v4u ld_sc1s(rsrc_t r, unsigned voff, unsigned soff) { return __builtin_amdgcn_raw_buffer_load_b128(r, voff, soff, 16); }
DEV v4u ld_s(rsrc_t r, unsigned voff, unsigned soff) { return __builtin_amdgcn_raw_buffer_load_b128(r, voff, soff, 0); }
DEV void lds_dma(rsrc_t r, unsigned char* ldsp, unsigned voff, unsigned soff) { __builtin_amdgcn_raw_ptr_buffer_load_lds(r, (LAS void*)ldsp, 16, voff, soff, 0, 0); }
DEV void st_sc1(rsrc_t r, unsigned off, v4u v) { __builtin_amdgcn_raw_buffer_store_b128(v, r, off, 0, 16); }
DEV unsigned ld32_sc1(rsrc_t r, unsigned off) { return __builtin_amdgcn_raw_buffer_load_b32(r, off, 0, 16); }
DEV void st32_sc1(rsrc_t r, unsigned off, unsigned v) { __builtin_amdgcn_raw_buffer_store_b32(v, r, off, 0, 16); }
DEV unsigned long long memtime() { unsigned long long t; asm volatile("s_memtime %0\n\ts_waitcnt lgkmcnt(0)" : "=s"(t) :: "memory"); return t; }
#define GUDELAY 24
#define ODELAY 0
#define MDELAY 12
#define TA1 1200u
#define TA2 700u
#define TA3 2800u
#define TDX 400u
#define TUNE_LOAD
DEV unsigned now32() { return (unsigned)__builtin_amdgcn_s_memtime(); }
DEV void wait_abs(unsigned t) { while ((int)(now32() - t) < 0) {} }
DEV v4u nosentu(v4u r) { v4u o = {min(r.x, 0xFFFFFFFEu), min(r.y, 0xFFFFFFFEu), min(r.z, 0xFFFFFFFEu), min(r.w, 0xFFFFFFFEu)}; return o; }
DEV v4u nosent(f32x4 r) { v4u o = {min(u_as(r[0]), 0xFFFFFFFEu), min(u_as(r[1]), 0xFFFFFFFEu), min(u_as(r[2]), 0xFFFFFFFEu), min(u_as(r[3]), 0xFFFFFFFEu)}; return o; }
#define BARRIER() asm volatile("s_waitcnt lgkmcnt(0)\n\ts_barrier" ::: "memory")
#define DRAIN_BARRIER() asm volatile("s_waitcnt vmcnt(0) lgkmcnt(0)\n\ts_barrier" ::: "memory")
#define Z4 ((f32x4){0.f, 0.f, 0.f, 0.f})
#define PIN2(a, b) asm volatile("" : "+v"(a), "+v"(b) :: "memory")

#define STAMPV(role, s, wave, k) do {} while (0)
#define STAMPDUMP(role) do {} while (0)
#define STAMPX(s, wave, k, val) do {} while (0)
#define STAMPG(slot, k) do {} while (0)
#define STAMPC(slot, k) do {} while (0)
#define STAMPP(k) do {} while (0)
#define STAMPW(slot, k) do {} while (0)

#define CP_DECL
#define CP(k) do {} while (0)
#define CP_DUMP(role) do {} while (0)
#define CPV(x) do {} while (0)
struct In {
  const u16 *Ax, *Awqkv, *Awphi, *Awo, *Bx, *Bmem0, *Wq, *Wk, *Wv, *Wo;
  u16* out; void* ws;
};

DEV v4u ld16_a8(const u16* p) { v2u a = *(const v2u*)p, b = *(const v2u*)(p + 4); return (v4u){a.x, a.y, b.x, b.y}; }

DEV void prep_parta_unit(const In& in, unsigned char* wb, int u, int lane) {
  const int rho = lane & 15, g = lane >> 4;
  if (u < 3 * NH * KS) {
    const int ks = u % KS, h = (u / KS) % NH, which = u / (KS * NH);
    v4u a[2], b[2];
#pragma unroll
    for (int t = 0; t < 2; ++t) {
      const int k = 32 * ks + sig(t, rho);
      const u16* src = in.Awqkv + (size_t)k * 1344 + which * 448 + h * 28 + 8 * g;
      v2u lo = *(const v2u*)src, hi = {0u, 0u};
      if (g < 3) hi = *(const v2u*)(src + 4);
      a[t] = (v4u){lo.x, lo.y, hi.x, hi.y};
    }
#pragma unroll
    for (int t = 0; t < 2; ++t) {
      const int f = sig(t, rho);
      unsigned e[8];
#pragma unroll
      for (int j = 0; j < 8; ++j) {
        const int d = 8 * g + j;
        unsigned v = 0;
        if (d < 28 && f < 28) v = which == 2 ? (d == f ? 0x3F80u : 0u) : (unsigned)in.Awphi[(h * 28 + d) * 28 + f];
        e[j] = v;
      }
      b[t] = (v4u){e[0] | (e[1] << 16), e[2] | (e[3] << 16), e[4] | (e[5] << 16), e[6] | (e[7] << 16)};
    }
    const float sc = which == 2 ? 1.f : 1.4426950408889634f;
    unsigned char* dst = wb + (which == 0 ? OFF_WQF : (which == 1 ? OFF_WKF : OFF_WVF));
#pragma unroll
    for (int t = 0; t < 2; ++t) {
      f32x4 d0 = mfma32(a[0], b[t], Z4), d1 = mfma32(a[1], b[t], Z4);
      *(v4u*)(dst + ((size_t)(h * 2 + t) * KS + ks) * 1024 + lane * 16) = pack8(d0 * sc, d1 * sc);
    }
  } else {
    const int v = u - 3 * NH * KS, np = v % KS, h = v / KS;
    v4u a[2];
#pragma unroll
    for (int t = 0; t < 2; ++t) {
      const int d = sig(t, rho);
      a[t] = (v4u){0u, 0u, 0u, 0u};
      if (d < 28) a[t] = *(const v4u*)(in.Awo + (size_t)(h * 28 + d) * DM + np * 32 + 8 * g);
    }
#pragma unroll
    for (int t = 0; t < 2; ++t) {
      const int n = sig(t, rho) - 8 * g;
      unsigned e[4] = {0u, 0u, 0u, 0u};
#pragma unroll
      for (int j = 0; j < 4; ++j) e[j] = (n == 2 * j ? 0x3F80u : 0u) | (n == 2 * j + 1 ? 0x3F800000u : 0u);
      const v4u b = {e[0], e[1], e[2], e[3]};
      f32x4 d0 = mfma32(a[0], b, Z4), d1 = mfma32(a[1], b, Z4);
      *(v4u*)(wb + OFF_WOF + (size_t)(h * CT + np * 2 + t) * 1024 + lane * 16) = pack8(d0, d1);
    }
  }
}

#define PAUX 16
#define PST(r, off, v) __builtin_amdgcn_raw_buffer_store_b128(v, r, off, 0, PAUX)
constexpr unsigned NFILL = 88;
constexpr int PREP_GRID = 256 + 128 + 37 + NFILL;
__global__ void __launch_bounds__(448, 4) k_prep(In in) {
  extern __shared__ __attribute__((aligned(16))) unsigned char lds[];
  unsigned char* wb = ws_base(in.ws);
  const int tid = threadIdx.x, lane = tid & 63, wave = rfl(tid >> 6), b = blockIdx.x;
  const int rho = lane & 15, g = lane >> 4;
  STAMPP(0);
  if (b >= 421) {
    const rsrc_t rw = mk_rsrc(wb, (unsigned)OFF_DYN), rd = mk_rsrc(wb + OFF_DYN, DYN_BYTES);
    const unsigned nt16 = NFILL * 448u * 16u, t0 = (unsigned)((b - 421) * 448 + tid);
    for (unsigned o = t0; o < (CTRL_BYTES - CT_TICK) / 4; o += NFILL * 448u) *(unsigned*)(wb + OFF_CTRL + CT_TICK + o * 4) = 0u;
    const v4u ones = {~0u, ~0u, ~0u, ~0u};
    for (unsigned o = t0 * 16u; o < XCH_PAIRS_USED * 16384u; o += nt16) {
      v4u v = ones;
      if (o < 16384) {
        const int w = (int)(o & 8191);
        if (w < 7168) {
          const int slot = w >> 7, cg = slot >> 1, rg = slot & 1, ii = (w >> 5) & 3, hf = (w >> 4) & 1;
          v = nosentu(*(const v4u*)(in.Bmem0 + (rg * 4 + ii) * DM + cg * 16 + hf * 8));
        }
      }
      PST(rw, (unsigned)OFF_XCH + o, v);
    }
    const unsigned RB = NH * KS * 512u;
    for (unsigned o = t0 * 16u; o < 2u * SEGB; o += nt16) { PST(rd, DY_GT + o, ones); PST(rd, DY_UT + o, ones); }
    for (unsigned o = t0 * 16u; o < 2u * RB; o += nt16) { PST(rd, DY_RC + 62u * RB + o, ones); PST(rd, DY_VC + 62u * RB + o, ones); }
    for (unsigned o = t0 * 16u; o < 65536u; o += nt16) PST(rd, DY_PX + 62u * 32768u + o, ones);
    for (unsigned o = t0 * 16u; o < 16384u; o += nt16) PST(rd, DY_PM + 62u * 8192u + o, ones);
    STAMPP(3);
    return;
  }
  if (b >= 384) {
    const int pi = (b - 384) * 7 + wave;
    if (lane == 0 && pi < 256) {
      const int pair = (pi * 3) >> 1;
      unsigned* p0 = (unsigned*)(wb + OFF_XCH + (size_t)pair * 16384 + 8064);
      unsigned* p1 = p0 + 2048;
      unsigned t0 = ~0u, t1 = ~0u;
      for (int rep = 0; rep < 3; ++rep) {
        unsigned v, v2, z = 0;
        unsigned long long a = memtime();
        asm volatile("global_atomic_or %0, %1, %2, off sc0\n\ts_waitcnt vmcnt(0)" : "=v"(v) : "v"(p0), "v"(z) : "memory");
        unsigned long long bb = memtime();
        asm volatile("global_atomic_or %0, %1, %2, off sc0\n\ts_waitcnt vmcnt(0)" : "=v"(v2) : "v"(p1), "v"(z) : "memory");
        unsigned long long c = memtime();
        t0 = min(t0, (unsigned)(bb - a)); t1 = min(t1, (unsigned)(c - bb));
      }
      const int vote = (t1 < t0) ^ (xcc_id() >= 4);
      const unsigned d = t0 > t1 ? t0 - t1 : t1 - t0;
      ((int*)(wb + OFF_CTRL + CT_VOTE))[pi] = (pair >> 7) * 4 + (d > 100 ? vote : 2);
    }
    STAMPP(3);
    return;
  }
  if (b >= 256) { prep_parta_unit(in, wb, (b - 256) * 7 + wave, lane); STAMPP(3); return; }
  const int mat = (b & 7) * 4 + (b >> 6), sub = (b >> 3) & 7;
  const int m = mat >> 4, h = mat & 15, rh = sub >> 2, cq = sub & 3;
  const rsrc_t rw = mk_rsrc(wb, (unsigned)OFF_DYN);
  const rsrc_t rq = mk_rsrc((void*)(m == 0 ? in.Wq : in.Wo), 448u * 4096u * 2u);
  const rsrc_t rp_ = mk_rsrc((void*)(m == 0 ? in.Wk : in.Wv), 448u * 4096u * 2u);
  const int rp = rh * 7 + wave;
  unsigned qv[8];
  if (m == 0) {
    const int T = cq * 7 + wave, k = 32 * (T >> 1) + sig(T & 1, rho);
#pragma unroll
    for (int es = 0; es < 8; ++es) qv[es] = (unsigned)(k * 4096 + h * EH + es * 32 + g * 8) * 2u;
  } else {
#pragma unroll
    for (int i = 0; i < 8; ++i) {
      const int n = ((i >> 2) * 28 + wave * 4 + (i & 3)) * 64 + lane, e = n / 14, ch = n % 14;
      qv[i] = (unsigned)((h * EH + e) * DM + cq * 112 + ch * 8) * 2u;
    }
  }
  const unsigned pv0 = (unsigned)((32 * rp + sig(0, rho)) * 4096 + h * EH + g * 8) * 2u, pv1 = pv0 + 4u * 4096u * 2u;
  v4u pa[2][8];
#pragma unroll
  for (int hf = 0; hf < 2; ++hf) {
#pragma unroll
    for (int i = 0; i < 4; ++i) {
      const int slot = m == 0 ? wave * 8 + hf * 4 + i : hf * 28 + wave * 4 + i;
      lds_dma(rq, lds + slot * 1024, qv[hf * 4 + i], 0u);
    }
#pragma unroll
    for (int i = 0; i < 4; ++i) { pa[0][hf * 4 + i] = ld_s(rp_, pv0, (hf * 4 + i) * 64u); pa[1][hf * 4 + i] = ld_s(rp_, pv1, (hf * 4 + i) * 64u); }
  }
  v4u xv;
  {
    const int j = b * 448 + tid, l = j & 63, f = j >> 6, cs = f % KS, tau = (f / KS) & 1, s = f / (2 * KS);
    xv = *(const v4u*)(in.Bx + (size_t)(s * 32 + sig(tau, l & 15)) * DM + cs * 32 + (l >> 4) * 8);
  }
  {
    const float sc = m ? 1.f : 0.0625f * 1.4426950408889634f;
    const unsigned dst = (unsigned)(m ? OFF_BTFR : OFF_AFR) + (unsigned)((h * CT + cq * 7) * KS + rp) * 1024u + lane * 16u;
    const unsigned troff = (unsigned)((8 * g + (rho >> 2)) * 224 + (rho & 3) * 8);
    DRAIN_BARRIER();
    STAMPP(1);
    if (m == 0) {
      v4u qb[2][8];
#pragma unroll
      for (int es = 0; es < 8; ++es) qb[0][es] = *(const v4u*)(lds + es * 1024 + lane * 16);
#pragma unroll
      for (int i = 0; i < 7; ++i) {
        f32x4 a0 = Z4, a1 = Z4;
#pragma unroll
        for (int es = 0; es < 8; ++es) {
          if (i + 1 < 7 && (es & 1) == 0) {
            qb[(i + 1) & 1][es] = *(const v4u*)(lds + ((i + 1) * 8 + es) * 1024 + lane * 16);
          }
          a0 = mfma32(pa[0][es], qb[i & 1][es], a0); a1 = mfma32(pa[1][es], qb[i & 1][es], a1);
          if (i + 1 < 7 && (es & 1) == 1) {
            qb[(i + 1) & 1][es] = *(const v4u*)(lds + ((i + 1) * 8 + es) * 1024 + lane * 16);
          }
        }
        PST(rw, dst + i * (KS * 1024u), pack8(a0 * sc, a1 * sc));
      }
    } else {
#pragma unroll
      for (int i = 0; i < 7; ++i) {
        f32x4 a0 = Z4, a1 = Z4;
#pragma unroll
        for (int es = 0; es < 8; ++es) {
          s16x4 lo = __builtin_amdgcn_ds_read_tr16_b64_v4i16((LAS s16x4*)(lds + troff + es * 32 * 224 + i * 32));
          s16x4 hi = __builtin_amdgcn_ds_read_tr16_b64_v4i16((LAS s16x4*)(lds + troff + (es * 32 + 4) * 224 + i * 32));
          v2u l2 = __builtin_bit_cast(v2u, lo), h2 = __builtin_bit_cast(v2u, hi);
          const v4u qb = {l2.x, l2.y, h2.x, h2.y};
          a0 = mfma32(pa[0][es], qb, a0); a1 = mfma32(pa[1][es], qb, a1);
        }
        PST(rw, dst + i * (KS * 1024u), pack8(a0, a1));
      }
    }
  }
  STAMPP(2);
  PST(rw, (unsigned)OFF_XFR + (unsigned)(b * 448 + tid) * 16u, xv);
  STAMPP(3);
}

DEV unsigned read_parity(const unsigned char* wb, int lane) {
  const int* v = (const int*)(wb + OFF_CTRL + CT_VOTE);
  int x[4];
#pragma unroll
  for (int k = 0; k < 4; ++k) x[k] = v[k * 64 + lane];
  unsigned par = 0;
#pragma unroll
  for (int p = 0; p < 3; ++p) {
    int n1 = 0, n0 = 0;
#pragma unroll
    for (int k = 0; k < 4; ++k) { n1 += __popcll(__ballot(x[k] == p * 4 + 1)); n0 += __popcll(__ballot(x[k] == p * 4)); }
    par |= (n1 > n0 ? 1u : 0u) << p;
  }
  return par;
}
DEV unsigned pair_off(unsigned par, int p) { return (unsigned)p * 16384u + (((par >> (p >> 7)) & 1u) << 13); }
DEV unsigned memx_off(unsigned par, int s) { return pair_off(par, s); }
DEV unsigned scx_off(unsigned par, int s, int rg, int h) { return pair_off(par, 64 + 4 * s + 2 * rg + (h >> 3)) + (h & 7) * 1024; }
DEV bool any_invalid(v4u v) { unsigned m = max(max(v.x, v.y), max(v.z, v.w)); return __any(m == 0xFFFFFFFFu); }
DEV v4u poll1(rsrc_t rx, unsigned off, int& budget) {
  v4u t;
#pragma nounroll
  do { asm volatile("" ::: "memory"); t = ld_sc1(rx, off); --budget; } while (any_invalid(t) && budget > 0);
  return t;
}
DEV unsigned simg_off(int c) {
  const int cg = c >> 4, rgi = (c >> 3) & 1, ii = (c >> 1) & 3, hf = c & 1;
  return (unsigned)((cg >> 2) * 1024 + (cg & 3) * 256 + (rgi * 4 + ii) * 32 + hf * 8);
}
DEV void simg_write(unsigned char* simg, unsigned off, v4u t) {
  *(v2u*)(simg + off) = (v2u){t.x, t.z};
  *(v2u*)(simg + off + 16) = (v2u){t.y, t.w};
}
template <bool NAT>
DEV void gather_chunk(rsrc_t rx, unsigned off, unsigned char* simg, unsigned soff, unsigned char* nat, int& budget) {
  v4u t = ld_sc1(rx, off);
  simg_write(simg, soff, t);
  if (NAT) *(v4u*)nat = t;
  if (__builtin_expect(any_invalid(t), 0)) {
    t = poll1(rx, off, budget);
    simg_write(simg, soff, t);
    if (NAT) *(v4u*)nat = t;
  }
}
DEV int min_lanes8(int v) {
  v = min(v, (int)dppu<DPP_XOR1>((unsigned)v)); v = min(v, (int)dppu<DPP_XOR2>((unsigned)v));
  return min(__builtin_amdgcn_readlane(v, 0), __builtin_amdgcn_readlane(v, 4));
}
DEV int wait_words(rsrc_t rc, unsigned off, int need, int& budget, bool sleep) {
  int mn;
  for (;;) {
    asm volatile("" ::: "memory");
    const int v = off == OOB ? 0x7fffffff : (int)ld32_sc1(rc, off);
    --budget;
    if (__all(v >= need) || budget <= 0) {
      mn = need;
      if (__all(v >= need + 16)) mn = need + 16; else if (__all(v >= need + 4)) mn = need + 4;
      break;
    }
    if (sleep) { __builtin_amdgcn_s_sleep(8); budget -= 4; }
  }
  return mn;
}

template <bool ISG>
DEV void gu_compute(const v4u (&W)[2][KS], const unsigned char* xb, v4u& r0, v4u& r1) {
  f32x4 a00 = Z4, a01 = Z4, a10 = Z4, a11 = Z4;
  v4u x0[KS], x1[KS];
#pragma unroll
  for (int k = 0; k < 2; ++k) { x0[k] = *(const v4u*)(xb + k * 1024); x1[k] = *(const v4u*)(xb + (KS + k) * 1024); }
  __builtin_amdgcn_sched_barrier(0);
#pragma unroll
  for (int k = 0; k < KS; ++k) {
    if (k + 2 < KS) { x0[k + 2] = *(const v4u*)(xb + (k + 2) * 1024); x1[k + 2] = *(const v4u*)(xb + (KS + k + 2) * 1024); }
    if (ISG) {
      a00 = mfma32(W[0][k], x0[k], a00); a10 = mfma32(W[1][k], x0[k], a10);
      a01 = mfma32(W[0][k], x1[k], a01); a11 = mfma32(W[1][k], x1[k], a11);
    } else {
      a00 = mfma32(x0[k], W[0][k], a00); a10 = mfma32(x0[k], W[1][k], a10);
      a01 = mfma32(x1[k], W[0][k], a01); a11 = mfma32(x1[k], W[1][k], a11);
    }
    __builtin_amdgcn_sched_barrier(0);
  }
  if (ISG) { r0 = pack8(a00, a10); r1 = pack8(a01, a11); } else { r0 = pack8(a00, a01); r1 = pack8(a10, a11); }
}

template <bool ISG>
DEV void gu_worker(unsigned char* wb, int w, unsigned char* lds, int tid) {
  const int lane = tid & 63, wave = rfl(tid >> 6), tp = wave & 3, sp = wave >> 2;
  const unsigned wfr = (unsigned)(ISG ? OFF_AFR : OFF_BTFR);
  const rsrc_t rw = mk_rsrc(wb, (unsigned)OFF_DYN);
  const rsrc_t rd = mk_rsrc(wb + OFF_DYN, DYN_BYTES), rc = mk_rsrc(wb + OFF_CTRL, CTRL_BYTES);
  const int T = 8 * (ISG ? w : w - 56) + 2 * tp;
  const int h = T / CT, tl = T % CT;
  auto stage = [&](int s, int buf) {
#pragma unroll
    for (int i = 0; i < 7; ++i)
      lds_dma(rw, lds + buf * 57344 + i * 8192 + wave * 1024, tid * 16u, (unsigned)OFF_XFR + (unsigned)s * 28672u + i * 8192u);
  };
  stage(0, 0);
  __builtin_amdgcn_sched_barrier(0);
  const unsigned o0 = (ISG ? (unsigned)((h * 2 + 0) * KS + (tl >> 1)) : (unsigned)T) * 1024u + lane * 16u;
  const unsigned o1 = (ISG ? (unsigned)((h * 2 + 1) * KS + (tl >> 1)) : (unsigned)(T + 1)) * 1024u + lane * 16u;
  if (sp == 1) {
    asm volatile("s_waitcnt vmcnt(0)\n\ts_barrier" ::: "memory");
    __builtin_amdgcn_s_sleep(GUDELAY);
  }
  v4u W[2][KS];
#pragma unroll
  for (int k = 0; k < KS; ++k)
#pragma unroll
    for (int t = 0; t < 2; ++t) W[t][k] = ld_s(rw, lane * 16u, wfr + (unsigned)((T + t) * KS + k) * 1024u);
  __builtin_amdgcn_sched_barrier(0);
  STAMPG(128 + w, 8); STAMPC(128 + w, 9);
  if (sp == 0) {
    asm volatile("s_waitcnt vmcnt(28)\n\ts_barrier" ::: "memory");
#pragma nounroll
    for (int sg = 0; sg < 2; ++sg) {
      v4u r0, r1;
      gu_compute<ISG>(W, lds + sg * 28672 + lane * 16, r0, r1);
      const unsigned ob = (ISG ? DY_GT : DY_UT) + (unsigned)sg * SEGB;
      st_sc1(rd, ob + o0, nosentu(r0)); st_sc1(rd, ob + o1, nosentu(r1));
    }
    STAMPG(128 + w, 11);
  }
  stage(2, 1);
  int buf = 1;
  for (int s = 2; s < NSEG; s += 2) {
    if (s <= 4) asm volatile("s_waitcnt vmcnt(0) lgkmcnt(0)\n\ts_barrier" ::: "memory");
    else asm volatile("s_waitcnt vmcnt(2) lgkmcnt(0)\n\ts_barrier" ::: "memory");
    if (wave == 7) st32_sc1(rc, lane == 0 ? (unsigned)(CT_PROG + w * 128) : OOB, (unsigned)(s <= 4 ? s : s - 2));
    stage(s + 2 < NSEG ? s + 2 : s, buf ^ 1);
    v4u r0, r1;
    gu_compute<ISG>(W, lds + buf * 57344 + sp * 28672 + lane * 16, r0, r1);
    const unsigned ob = (ISG ? DY_GT : DY_UT) + (unsigned)(s + sp) * SEGB;
    st_sc1(rd, ob + o0, nosentu(r0)); st_sc1(rd, ob + o1, nosentu(r1));
    buf ^= 1;
  }
  DRAIN_BARRIER();
  if (wave == 7) st32_sc1(rc, lane == 0 ? (unsigned)(CT_PROG + w * 128) : OOB, (unsigned)NSEG);
  STAMPG(128 + w, 5); STAMPC(128 + w, 10);
}

template <bool HASG>
DEV void role_S(unsigned char* wb, int h, int q, unsigned char* lds, int tid) {
  const int lane = tid & 63, wave = rfl(tid >> 6);
  TUNE_LOAD
  unsigned char* rimg = lds;
  unsigned char* mimg = lds + 4096;
  unsigned char* simg = lds + 12288;
  unsigned char* gring = lds + 20480;
  const rsrc_t rx = mk_rsrc(wb + OFF_XCH, (unsigned)XCH_BYTES);
  int budget = SPIN_BUDGET;
  const unsigned soff = simg_off(tid);
  if (HASG && wave >= 4) {
    const rsrc_t rd = mk_rsrc(wb + OFF_DYN, DYN_BYTES);
    const int f0 = (wave - 4) * 7;
    const unsigned src = DY_GT + (unsigned)((h * 2 + q) * KS + (f0 % KS)) * 1024u + (unsigned)(f0 / KS) * SEGB;
    v4u t[7];
    bool bad;
#pragma nounroll
    do {
      asm volatile("" ::: "memory");
      unsigned m = 0;
#pragma unroll
      for (int k = 0; k < 7; ++k) t[k] = ld_sc1s(rd, lane * 16u, src + k * 1024u);
#pragma unroll
      for (int k = 0; k < 7; ++k) m = max(m, max(max(t[k].x, t[k].y), max(t[k].z, t[k].w)));
      bad = __any(m == 0xFFFFFFFFu);
      budget -= 2;
    } while (bad && budget > 0);
#pragma unroll
    for (int k = 0; k < 7; ++k) *(v4u*)(gring + (f0 + k) * 1024 + lane * 16) = t[k];
    if (wave == 7) STAMPW(h * 4 + q, 12);
  }
  if (wave == 7) {
    if (!HASG) { BARRIER(); return; }
    const rsrc_t rd = mk_rsrc(wb + OFF_DYN, DYN_BYTES), rc = mk_rsrc(wb + OFF_CTRL, CTRL_BYTES);
    const int w0 = (28 * h) >> 3, w1 = (28 * h + 27) >> 3;
    const unsigned poff = (w0 + lane <= w1) ? (unsigned)(CT_PROG + (w0 + lane) * 128) : OOB;
    const unsigned gsrc = DY_GT + (unsigned)((h * 2 + q) * KS) * 1024u;
    int known = 0, pv = 0;
    auto fetch = [&](int s) {
      known = max(known, min_lanes8(poff == OOB ? 0x7fffffff : pv));
      if (known < s + 1) known = wait_words(rc, poff, s + 1, budget, false);
      pv = (int)ld32_sc1(rc, known < NSEG ? poff : OOB);
      v4u t[KS];
#pragma unroll
      for (int k = 0; k < KS; ++k) t[k] = ld_sc1s(rd, lane * 16u, gsrc + (unsigned)s * SEGB + k * 1024);
#pragma unroll
      for (int k = 0; k < KS; ++k) *(v4u*)(gring + (s & 1) * 14336 + k * 1024 + lane * 16) = t[k];
    };
    BARRIER();
    for (int s = 0; s < NSEG; ++s) {
      if (s > 0 && s + 1 < NSEG - 1) { __builtin_amdgcn_s_sleep(10); fetch(s + 1); }
      BARRIER();
      BARRIER();
    }
    return;
  }
  if (wave >= 4) {
    const unsigned par = read_parity(wb, lane);
    BARRIER();
    unsigned tanch = now32() - 100000u;
    for (int s = 0; s < NSEG; ++s) {
      unsigned moff = memx_off(par, s) + tid * 16; asm volatile("" : "+v"(moff));
      wait_abs(tanch + TA3);
      STAMPV(h * 4 + q, s, wave, 2);
      const int b0 = budget;
      gather_chunk<true>(rx, moff, simg, soff, mimg + tid * 16, budget);
      STAMPV(h * 4 + q, s, wave, 0);
      STAMPX(s, wave, 1, 1 + b0 - budget);
      BARRIER();
      BARRIER();
      tanch = now32();
    }
    STAMPDUMP(h * 4 + q);
    return;
  }
  const int cp0 = q == 0 ? 0 : (q == 1 ? 3 : (q == 2 ? 6 : 10));
  const int i = lane & 15, g = lane >> 4, i8 = i & 7;
  const unsigned char* fbase = mimg + (g >> 1) * 256 + (i8 >> 2) * 128 + (i8 & 3) * 32 + (g & 1) * 16;
  const bool act = i < 8;
  if (HASG && wave == 3) {
    const unsigned par = read_parity(wb, lane);
    BARRIER();
    const unsigned pl = (unsigned)(512 + q * 256 + (i & 3) * 64 + g * 16);
    unsigned tanch = now32() - 100000u;
    for (int s = 0; s < NSEG - 1; ++s) {
      v4u G[KS];
      if (s > 0) __builtin_amdgcn_s_sleep(6);
#pragma unroll
      for (int k = 0; k < KS; ++k) G[k] = *(const v4u*)(gring + (s & 1) * 14336 + k * 1024 + lane * 16);
      unsigned pubo = act ? scx_off(par, s, i >> 2, h) + pl : OOB;
      unsigned moff = memx_off(par, s) + tid * 16; asm volatile("" : "+v"(moff), "+v"(pubo));
      wait_abs(tanch + TA3);
      gather_chunk<true>(rx, moff, simg, soff, mimg + tid * 16, budget);
      BARRIER();
      f32x4 acc = Z4;
      v4u F[KS];
#pragma unroll
      for (int k = 0; k < 6; ++k) F[k] = *(const v4u*)(fbase + k * 512);
      __builtin_amdgcn_sched_barrier(0);
#pragma unroll
      for (int k = 0; k < KS; ++k) {
        if (k + 6 < KS) F[k + 6] = *(const v4u*)(fbase + (k + 6) * 512);
        acc = mfma32(G[k], F[k], acc);
        __builtin_amdgcn_sched_barrier(0);
      }
      st_sc1(rx, pubo, nosent(acc));
      STAMPV(h * 4 + q, s, wave, 2);
      BARRIER();
      tanch = now32();
    }
    {
      wait_abs(tanch + TA3);
      gather_chunk<true>(rx, memx_off(par, NSEG - 1) + tid * 16, simg, soff, mimg + tid * 16, budget);
      BARRIER();
      BARRIER();
    }
    STAMPDUMP(h * 4 + q);
    return;
  }
  constexpr int NW = HASG ? 3 : 4;
  const int cp = cp0 + wave;
  const rsrc_t rw = mk_rsrc(wb, (unsigned)OFF_DYN), rd = mk_rsrc(wb + OFF_DYN, DYN_BYTES);
  v8u W[2][7];
#pragma unroll
  for (int t = 0; t < 2; ++t)
#pragma unroll
    for (int k = 0; k < 7; ++k) {
      const unsigned o = (unsigned)OFF_AFR + (unsigned)((h * CT + 2 * cp + t) * KS + 2 * k) * 1024u;
      W[t][k] = cat8(ld_s(rw, lane * 16u, o), ld_s(rw, lane * 16u, o + 1024u));
    }
  __builtin_amdgcn_sched_barrier(0);
  const unsigned par = read_parity(wb, lane);
  const unsigned char* abase = simg + g * 256 + (2 * (i >> 2) + (i & 1)) * 32 + ((i >> 1) & 1) * 16;
  const int sidx = (i & 2) ? 0xEEEE : 0x4444;
  unsigned char* rwr = rimg + (wave * 32 + sig(0, i)) * 16 + g * 4;
  const unsigned trbase = (unsigned)((8 * g + (i >> 2)) * 16 + (i & 1) * 8);
  const unsigned pub_l = (unsigned)(q * 128 + (i & 3) * 32 + (g & 1) * 16);
  const bool pact = act && g < 2;
  const unsigned rlane = DY_RC + (unsigned)(h * KS + cp) * 512u + (unsigned)(g * 8 + i) * 16u;
  auto trfrag = [&](int p) {
    s16x4 lo = __builtin_amdgcn_ds_read_tr16_b64_v4i16((LAS s16x4*)(rimg + trbase + p * 512));
    s16x4 hi = __builtin_amdgcn_ds_read_tr16_b64_v4i16((LAS s16x4*)(rimg + trbase + p * 512 + 64));
    v2u l2 = __builtin_bit_cast(v2u, lo), h2 = __builtin_bit_cast(v2u, hi);
    return (v4u){l2.x, l2.y, h2.x, h2.y};
  };
  if (wave == 0) STAMPW(h * 4 + q, 11);
  BARRIER();
  CP_DECL
  unsigned tanch = now32() - 100000u;
  for (int s = 0; s < NSEG; ++s) {
    unsigned pubo = (pact && s < NSEG - 1) ? scx_off(par, s, i >> 2, h) + pub_l : OOB;
    unsigned moff = memx_off(par, s) + tid * 16; asm volatile("" : "+v"(moff), "+v"(pubo));
    wait_abs(tanch + TA3);
    CP(0);
    gather_chunk<true>(rx, moff, simg, soff, mimg + tid * 16, budget);
    CP(1);
    BARRIER();
    CP(2);
    f32x4 a0 = Z4, a1 = Z4;
    v4u F[7], Fc[NW];
#pragma unroll
    for (int k = 0; k < 4; ++k) F[k] = *(const v4u*)(abase + k * 1024);
    __builtin_amdgcn_sched_barrier(0);
#pragma unroll
    for (int k = 0; k < 7; ++k) {
      if (k + 4 < 7) F[k + 4] = *(const v4u*)(abase + (k + 4) * 1024);
      if (k >= 3 && k - 3 < NW) Fc[k - 3] = *(const v4u*)(fbase + (cp0 + k - 3) * 512);
      a0 = smfma64(F[k], W[0][k], a0, sidx); a1 = smfma64(F[k], W[1][k], a1, sidx);
      __builtin_amdgcn_sched_barrier(0);
    }
    STAMPV(h * 4 + q, s, wave, 0);
    *(unsigned*)(rwr) = pk2(a0[0] + a0[2], a0[1] + a0[3]);
    *(unsigned*)(rwr + 64) = pk2(a1[0] + a1[2], a1[1] + a1[3]);
    STAMPV(h * 4 + q, s, wave, 1);
    CP(3);
    BARRIER();
    CP(4);
    if (wave == 0) {
      v4u rf[NW];
#pragma unroll
      for (int p = 0; p < NW; ++p) rf[p] = trfrag(p);
      __builtin_amdgcn_sched_barrier(0);
      f32x4 acc2 = Z4;
#pragma unroll
      for (int p = 0; p < NW; ++p) acc2 = mfma32(rf[p], Fc[p], acc2);
      st_sc1(rx, pubo, nosent(acc2));
    }
    STAMPV(h * 4 + q, s, wave, 2);
    CP(5);
    tanch = now32() - (wave == 0 ? 200u : 0u);
    if (s < NSEG - 1) __builtin_amdgcn_s_sleep(8);
    const v4u rpk = trfrag(wave);
    st_sc1(rd, act ? rlane + (unsigned)s * (NH * KS * 512u) : OOB, nosentu(rpk));
  }
  CP_DUMP(h * 4 + q);
  if (budget <= 0) *(GAS unsigned*)(wb + OFF_CTRL + CT_ABORT) = 1u;
  STAMPDUMP(h * 4 + q);
}

DEV void role_O(unsigned char* wb, int rg, int cg, unsigned char* lds, int tid) {
  TUNE_LOAD
  const int lane = tid & 63, wave = rfl(tid >> 6);
  const int rho = lane & 15, g = lane >> 4;
  const rsrc_t rx = mk_rsrc(wb + OFF_XCH, (unsigned)XCH_BYTES), rd = mk_rsrc(wb + OFF_DYN, DYN_BYTES), rc = mk_rsrc(wb + OFF_CTRL, CTRL_BYTES);
  int budget = SPIN_BUDGET;
  const rsrc_t rw = mk_rsrc(wb, (unsigned)OFF_DYN);
  for (int i = 0; i < ODELAY; ++i) __builtin_amdgcn_s_sleep(16);
  v8u W[2][7]; v4u U[2], Un[2];
#pragma unroll
  for (int hh = 0; hh < 2; ++hh)
#pragma unroll
    for (int k = 0; k < 7; ++k) {
      const unsigned o = (unsigned)OFF_BTFR + (unsigned)(((2 * wave + hh) * CT + cg) * KS + 2 * k) * 1024u;
      W[hh][k] = cat8(ld_s(rw, lane * 16u, o), ld_s(rw, lane * 16u, o + 1024u));
    }
  __builtin_amdgcn_sched_barrier(0);
  const unsigned par = read_parity(wb, lane);
  const unsigned ubase = DY_UT + (unsigned)((2 * wave) * CT + cg) * 1024u;
  const unsigned poff = lane < 2 ? (unsigned)(CT_PROG + (56 + (((2 * wave + lane) * CT + cg) >> 3)) * 128) : OOB;
  int known = 0;
  if (wave == 0) STAMPW(64 + cg * 2 + rg, 11);
  U[0] = poll1(rd, ubase + lane * 16u, budget); U[1] = poll1(rd, ubase + CT * 1024 + lane * 16u, budget);
  if (wave == 0) STAMPW(64 + cg * 2 + rg, 12);
  unsigned char* mimg = lds;
  float* red = (float*)(lds + 8192);
  const unsigned aoff = (unsigned)(g * 256 + ((rho >> 2) + 4 * (rho & 1)) * 32 + ((rho >> 1) & 1) * 16);
  const int sidx = (rho & 2) ? 0xEEEE : 0x4444;
  const unsigned soff = simg_off(tid);
  const int u = rho >> 3, hh_l = (rho >> 2) & 1, ii = rho & 3;
  const int hl = 2 * wave + hh_l;
  const unsigned off_mm = (unsigned)(g * 128 + ii * 32 + u * 16);
  const unsigned off_mx = (unsigned)(512 + u * 256 + ii * 64 + g * 16);
  const int wpos = (g + 4 * ii) * 20 + hl + (u ? 1280 : 0);
  const int rpos = (((lane & 15) >> 2) + 4 * (lane >> 4) + 16 * (lane & 3)) * 20;
  const unsigned publ = (unsigned)((cg * 2 + rg) * 128 + lane * 2);
  const unsigned stepo = (wave == 7 && lane == 0 && cg == 0 && rg == 0) ? (unsigned)CT_STEP : OOB;

  const bool vact = rg == 0 && !(g & 1);
  const unsigned vlane = DY_VC + (unsigned)((wave >> 1) * CT + cg) * 1024u + (unsigned)(((2 * wave + (g >> 1)) & 3) * 16 + rho) * 16u;
  v4u vprev = {0u, 0u, 0u, 0u};
  const int nstep = rg == 0 ? NSEG : NSEG - 1;
  CP_DECL
  unsigned tanch = now32() - 100000u;
  for (int s = 0; s < nstep; ++s) {
    {
      wait_abs(tanch + TA2);
      if (wave < 7) {
        gather_chunk<false>(rx, memx_off(par, s) + tid * 16, mimg, soff, nullptr, budget);
      }
    }
    BARRIER();
    tanch = now32();
    STAMPV(64 + cg * 2 + rg, s, wave, 0);
    {
      const int sn = min(s + 1, NSEG - 2);
      if (__builtin_expect(known < sn + 1, 0)) known = wait_words(rc, poff, sn + 1, budget, false);
      Un[0] = ld_sc1s(rd, lane * 16u, ubase + (unsigned)sn * SEGB); Un[1] = ld_sc1s(rd, lane * 16u, ubase + (unsigned)sn * SEGB + CT * 1024);
      st32_sc1(rc, stepo, (unsigned)s);
      st_sc1(rd, (vact && s > 0) ? vlane + (unsigned)(s - 1) * (4u * CT * 1024u) : OOB, vprev);
    }
    __builtin_amdgcn_sched_barrier(0);
    f32x4 av0 = Z4, av1 = Z4;
    v4u af[7];
#pragma unroll
    for (int k = 0; k < 4; ++k) af[k] = *(const v4u*)(mimg + k * 1024 + aoff);
    __builtin_amdgcn_sched_barrier(0);
#pragma unroll
    for (int k = 0; k < 7; ++k) {
      if (k + 4 < 7) af[k + 4] = *(const v4u*)(mimg + (k + 4) * 1024 + aoff);
      av0 = smfma64(af[k], W[0][k], av0, sidx); av1 = smfma64(af[k], W[1][k], av1, sidx);
      __builtin_amdgcn_sched_barrier(0);
    }
    v4u vfc = {pk2(av0[0] + av0[2], av0[1] + av0[3]), pk2(av1[0] + av1[2], av1[1] + av1[3]), 0u, 0u};
    {
      auto r = __builtin_amdgcn_permlane32_swap(vfc.x, vfc.y, false, false);
      auto a = __builtin_amdgcn_permlane16_swap(r[0], r[0], false, false);
      auto b = __builtin_amdgcn_permlane16_swap(r[1], r[1], false, false);
      vprev = nosentu((v4u){a[0], a[1], b[0], b[1]});
    }
    if (s == NSEG - 1) { st_sc1(rd, vact ? vlane + (unsigned)s * (4u * CT * 1024u) : OOB, vprev); break; }
    const unsigned sb = scx_off(par, s, rg, hl);
    unsigned pubo = (lane & 1) ? OOB : memx_off(par, s + 1) + publ;
    unsigned om = sb + off_mm, ox = sb + off_mx;
    v4u bm = {0u, 0u, 0u, 0u};
    asm volatile("" : "+v"(om), "+v"(ox), "+v"(pubo), "+v"(vfc), "+v"(bm), "+v"(vprev));
    const unsigned tsc = tanch + TA1;
    wait_abs(tsc - TDX);
    v4u tx = ld_sc1(rx, ox);
    wait_abs(tsc);
    STAMPV(64 + cg * 2 + rg, s, wave, 1);
    const int b1 = budget;
    CP(0);
    v4u tm = ld_sc1(rx, om);
    __builtin_amdgcn_sched_barrier(0);
    if (__builtin_expect(any_invalid(tx), 0)) tx = poll1(rx, ox, budget);
    const float x0 = f_as(tx.x), x1 = f_as(tx.y), x2 = f_as(tx.z), x3 = f_as(tx.w);
    float mx = vmax(vmax(x0, x1), vmax(x2, x3));
    mx = vmax(mx, dppf<DPP_ROR8>(mx));
    mx = xg_max(mx);
    const float e0 = __builtin_amdgcn_exp2f(x0 - mx), e1 = __builtin_amdgcn_exp2f(x1 - mx), e2 = __builtin_amdgcn_exp2f(x2 - mx), e3 = __builtin_amdgcn_exp2f(x3 - mx);
    const unsigned q01 = pk2(e0, e1), q23 = pk2(e2, e3);
    const v4u bx = {q01, q23, dppu<DPP_ROR8>(q01), dppu<DPP_ROR8>(q23)};
    f32x4 o0 = mfma32(U[0], bx, Z4), o1 = mfma32(U[1], bx, Z4);
    float lx = (e0 + e1) + (e2 + e3);
    lx += dppf<DPP_ROR8>(lx);
    lx = xg_sum(lx);
    f32x4 ox4 = hh_l ? o1 : o0;
    asm volatile("" : "+v"(ox4), "+v"(lx), "+v"(mx));
    __builtin_amdgcn_sched_barrier(0);
    if (__builtin_expect(any_invalid(tm), 0)) tm = poll1(rx, om, budget);
    if (wave) STAMPX(s, wave, 3, 1 + b1 - budget);
    STAMPV(64 + cg * 2 + rg, s, wave, 2);
    CP(1);
    auto p0 = __builtin_amdgcn_permlane32_swap(tm.x, tm.z, false, false);
    auto p1 = __builtin_amdgcn_permlane32_swap(tm.y, tm.w, false, false);
    float a0 = f_as(p0[0]) + f_as(p0[1]), a1 = f_as(p1[0]) + f_as(p1[1]);
    auto p2 = __builtin_amdgcn_permlane16_swap(u_as(a0), u_as(a1), false, false);
    const float sm = f_as(p2[0]) + f_as(p2[1]);
    float m = vmax(vmax(sm, dppf<DPP_ROR8>(sm)), mx);
    m = xg_max(m);
    CPV(m); CP(2);
    const float em = __builtin_amdgcn_exp2f(sm - m), fx = __builtin_amdgcn_exp2f(mx - m);
    const float em2 = dppf<DPP_ROR8>(em);
    const unsigned pe = pk2(em, em2);
    bm.x = hh_l ? 0u : pe; bm.y = hh_l ? pe : 0u;
    CPV(bm); CP(3);
    ox4 = mfma32(vfc, bm, ox4 * fx);
    const float l = xg_sum(em + em2) + fx * lx;
    const float il = __builtin_amdgcn_rcpf(l);
#pragma unroll
    for (int r = 0; r < 4; ++r) red[wpos + 320 * r] = ox4[r] * il;
    CP(4);
    BARRIER();
    CP(5);
    if (wave == 0) {
      f32x4 a = *(const f32x4*)(red + rpos), b = *(const f32x4*)(red + rpos + 4), c = *(const f32x4*)(red + rpos + 8), d = *(const f32x4*)(red + rpos + 12);
      f32x4 r4 = (a + b) + (c + d);
      float v = (r4[0] + r4[1]) + (r4[2] + r4[3]);
      st32_sc1(rx, pubo, min(pk2(v, dppf<DPP_XOR1>(v)), 0xFFFFFFFEu));
    }
    tanch = now32() - (wave == 0 ? 200u : 0u);
    if (wave == 0) STAMPV(64 + cg * 2 + rg, s, wave, 3);
    CP(6);
    U[0] = Un[0]; U[1] = Un[1];
    if (known < NSEG) {
      const int pv = poff == OOB ? 0x7fffffff : (int)ld32_sc1(rc, poff);
      known = rfl(max(known, min_lanes8(pv)));
    }
  }
  CP_DUMP(64 + cg * 2 + rg);
  if (budget <= 0) *(GAS unsigned*)(wb + OFF_CTRL + CT_ABORT) = 1u;
  STAMPDUMP(64 + cg * 2 + rg);
}

DEV float row16_max(float v) {
  v = vmax(v, dppf<DPP_XOR1>(v)); v = vmax(v, dppf<DPP_XOR2>(v));
  v = vmax(v, dppf<DPP_ROR4>(v)); v = vmax(v, dppf<DPP_ROR8>(v)); return v;
}
DEV float row16_sum(float v) {
  v += dppf<DPP_XOR1>(v); v += dppf<DPP_XOR2>(v); v += dppf<DPP_ROR4>(v); v += dppf<DPP_ROR8>(v); return v;
}
DEV int ticket(unsigned char* wb, int ctr, unsigned char* lds, int tid) {
  volatile LAS int* sl = (volatile LAS int*)(lds + 114688 - 16);
  __syncthreads();
  if (tid == 0) sl[0] = (int)__hip_atomic_fetch_add((GAS unsigned*)(wb + OFF_CTRL + ctr), 1u, __ATOMIC_RELAXED, __HIP_MEMORY_SCOPE_AGENT);
  __syncthreads();
  return rfl(sl[0]);
}
DEV void parta_unit1(const In& in, unsigned char* wb, int u, unsigned char* lds, int tid) {
  const int lane = tid & 63, wave = rfl(tid >> 6);
  const int rho = lane & 15, g = lane >> 4;
  const int h = u & 15, part = u >> 4;
  const rsrc_t rd = mk_rsrc(wb + OFF_DYN, DYN_BYTES), rw = mk_rsrc(wb, (unsigned)OFF_DYN);
#pragma unroll
  for (int i = 0; i < 7; ++i) {
    const int f = i * 8 + wave;
    const unsigned src = f < 28 ? (unsigned)OFF_WKF + h * 28672u + f * 1024u : (unsigned)OFF_WVF + h * 28672u + (f - 28) * 1024u;
    lds_dma(rw, lds + f * 1024, lane * 16u, src);
  }
  const int s = part * 8 + wave;
  const u16* xp0 = in.Ax + (size_t)(s * 32 + rho) * DM + g * 8;
  const u16* xp1 = xp0 + 16 * DM;
  v4u X0[KS], X1[KS];
#pragma unroll
  for (int k = 0; k < KS; ++k) { X0[k] = *(const v4u*)(xp0 + k * 32); X1[k] = *(const v4u*)(xp1 + k * 32); }
  DRAIN_BARRIER();
  f32x4 pk_[2][2], vv[2][2];
#pragma unroll
  for (int t = 0; t < 2; ++t)
#pragma unroll
    for (int b = 0; b < 2; ++b) { pk_[t][b] = Z4; vv[t][b] = Z4; }
#pragma unroll
  for (int k = 0; k < KS; ++k) {
    const v4u k0 = *(const v4u*)(lds + k * 1024 + lane * 16), k1 = *(const v4u*)(lds + (KS + k) * 1024 + lane * 16);
    const v4u v0 = *(const v4u*)(lds + 28672 + k * 1024 + lane * 16), v1 = *(const v4u*)(lds + 28672 + (KS + k) * 1024 + lane * 16);
    pk_[0][0] = mfma32(X0[k], k0, pk_[0][0]); pk_[0][1] = mfma32(X0[k], k1, pk_[0][1]);
    pk_[1][0] = mfma32(X1[k], k0, pk_[1][0]); pk_[1][1] = mfma32(X1[k], k1, pk_[1][1]);
    vv[0][0] = mfma32(X0[k], v0, vv[0][0]); vv[0][1] = mfma32(X0[k], v1, vv[0][1]);
    vv[1][0] = mfma32(X1[k], v0, vv[1][0]); vv[1][1] = mfma32(X1[k], v1, vv[1][1]);
  }
  const bool pad = rho >= 12;
  f32x4 pp[2][2], pn[2][2];
#pragma unroll
  for (int t = 0; t < 2; ++t)
#pragma unroll
    for (int r = 0; r < 4; ++r) {
      const float a0 = pk_[t][0][r], a1 = pad ? a0 : pk_[t][1][r];
      const float mx = row16_max(vmax(a0, a1));
      const float mn = -row16_max(vmax(-a0, -a1));
      const float ep0 = __builtin_amdgcn_exp2f(a0 - mx), ep1 = pad ? 0.f : __builtin_amdgcn_exp2f(a1 - mx);
      const float en0 = __builtin_amdgcn_exp2f(mn - a0), en1 = pad ? 0.f : __builtin_amdgcn_exp2f(mn - a1);
      const float ip = __builtin_amdgcn_rcpf(row16_sum(ep0 + ep1)), in_ = __builtin_amdgcn_rcpf(row16_sum(en0 + en1));
      pp[t][0][r] = ep0 * ip; pp[t][1][r] = ep1 * ip; pn[t][0][r] = en0 * in_; pn[t][1][r] = en1 * in_;
    }
  if (rho == 12) { vv[0][1] = (f32x4){1.f, 1.f, 1.f, 1.f}; vv[1][1] = (f32x4){1.f, 1.f, 1.f, 1.f}; }
  const v4u vb0 = pack8(vv[0][0], vv[1][0]), vb1 = pack8(vv[0][1], vv[1][1]);
  v4u fa[4] = {pack8(pp[0][0], pp[1][0]), pack8(pp[0][1], pp[1][1]), pack8(pn[0][0], pn[1][0]), pack8(pn[0][1], pn[1][1])};
  BARRIER();
  float* red = (float*)lds;
#pragma unroll
  for (int a = 0; a < 4; ++a) {
    *(f32x4*)(red + ((wave * 8 + a * 2 + 0) * 64 + lane) * 4) = mfma32(fa[a], vb0, Z4);
    *(f32x4*)(red + ((wave * 8 + a * 2 + 1) * 64 + lane) * 4) = mfma32(fa[a], vb1, Z4);
  }
  BARRIER();
  {
    f32x4 sum = Z4;
#pragma unroll
    for (int w = 0; w < 8; ++w) sum += *(const f32x4*)(red + (w * 512 + tid) * 4);
    st_sc1(rd, DY_KVP + (unsigned)(part * NH + h) * 8192u + tid * 16u, __builtin_bit_cast(v4u, sum));
  }
  DRAIN_BARRIER();
  const int arrived = ticket(wb, CT_HCNT + h * 128, lds, tid);
  if (arrived == 7) {
    if (wave < 4) {
      const int sg = wave >> 1, b = wave & 1;
      v4u lo[8], hi[8];
#pragma unroll
      for (int p = 0; p < 8; ++p) {
        lo[p] = ld_sc1s(rd, lane * 16u, DY_KVP + (unsigned)(p * NH + h) * 8192u + (unsigned)((sg * 2 + 0) * 2 + b) * 1024u);
        hi[p] = ld_sc1s(rd, lane * 16u, DY_KVP + (unsigned)(p * NH + h) * 8192u + (unsigned)((sg * 2 + 1) * 2 + b) * 1024u);
      }
      f32x4 l4 = Z4, h4 = Z4;
#pragma unroll
      for (int p = 0; p < 8; ++p) { l4 += __builtin_bit_cast(f32x4, lo[p]); h4 += __builtin_bit_cast(f32x4, hi[p]); }
      st_sc1(rd, DY_KV + (unsigned)(h * 4 + wave) * 1024u + lane * 16u, pack8(l4, h4));
    }
    DRAIN_BARRIER();
    if (tid == 0) __hip_atomic_fetch_add((GAS unsigned*)(wb + OFF_CTRL + CT_ACNT), 1u, __ATOMIC_RELAXED, __HIP_MEMORY_SCOPE_AGENT);
  }
}

DEV void parta_unit2(const In& in, unsigned char* wb, int u, unsigned char* lds, int tid) {
  const int lane = tid & 63, wave = rfl(tid >> 6);
  const int rho = lane & 15, g = lane >> 4;
  const rsrc_t rd = mk_rsrc(wb + OFF_DYN, DYN_BYTES), rc = mk_rsrc(wb + OFF_CTRL, CTRL_BYTES);
  const rsrc_t rw = mk_rsrc(wb, (unsigned)OFF_DYN);
  int budget = SPIN_BUDGET;
#pragma unroll
  for (int i = 0; i < 4; ++i) {
    const int f = i * 8 + wave;
    if (f < 28) {
      const int tt = f / KS, ks = f % KS;
      __builtin_amdgcn_global_load_lds((const unsigned*)(in.Ax + (size_t)(u * 32 + tt * 16 + rho) * DM + ks * 32 + g * 8),
                                       (LAS unsigned*)(lds + f * 1024), 16, 0, 0);
    }
  }
  unsigned char* ob_l = lds + 28672;
  f32x4 p[2][2][2];
#pragma unroll
  for (int hh = 0; hh < 2; ++hh) {
    const unsigned wq = (unsigned)OFF_WQF + (unsigned)(2 * wave + hh) * 28672u;
    v4u Wf[2][KS];
    asm volatile("" ::: "memory");
#pragma unroll
    for (int k = 0; k < KS; ++k) { Wf[0][k] = ld_s(rw, lane * 16u, wq + k * 1024); Wf[1][k] = ld_s(rw, lane * 16u, wq + (KS + k) * 1024); }
    if (hh == 0) DRAIN_BARRIER();
    p[hh][0][0] = Z4; p[hh][0][1] = Z4; p[hh][1][0] = Z4; p[hh][1][1] = Z4;
#pragma unroll
    for (int k = 0; k < KS; ++k) {
      const v4u x0 = *(const v4u*)(lds + k * 1024 + lane * 16), x1 = *(const v4u*)(lds + (KS + k) * 1024 + lane * 16);
      p[hh][0][0] = mfma32(Wf[0][k], x0, p[hh][0][0]); p[hh][1][0] = mfma32(Wf[1][k], x0, p[hh][1][0]);
      p[hh][0][1] = mfma32(Wf[0][k], x1, p[hh][0][1]); p[hh][1][1] = mfma32(Wf[1][k], x1, p[hh][1][1]);
    }
    PIN2(p[hh][0][0], p[hh][0][1]); PIN2(p[hh][1][0], p[hh][1][1]);
  }
  wait_words(rc, lane == 0 ? (unsigned)CT_ACNT : OOB, NH, budget, true);
  const bool pad = g == 3;
#pragma unroll
  for (int hh = 0; hh < 2; ++hh) {
    const int h = 2 * wave + hh;
    v4u kvf[4];
#pragma unroll
    for (int a = 0; a < 4; ++a) kvf[a] = ld_sc1s(rd, lane * 16u, DY_KV + (unsigned)(h * 4 + a) * 1024u);
#pragma unroll
    for (int tt = 0; tt < 2; ++tt) {
      const f32x4 p0 = p[hh][0][tt], p1 = pad ? p0 : p[hh][1][tt];
      float mx = vmax(vmax(vmax(p0[0], p0[1]), vmax(p0[2], p0[3])), vmax(vmax(p1[0], p1[1]), vmax(p1[2], p1[3])));
      float mn = vmax(vmax(vmax(-p0[0], -p0[1]), vmax(-p0[2], -p0[3])), vmax(vmax(-p1[0], -p1[1]), vmax(-p1[2], -p1[3])));
      mx = xg_max(mx); mn = -xg_max(mn);
      f32x4 ep0, ep1, en0, en1;
      float sp = 0.f, sn = 0.f;
#pragma unroll
      for (int r = 0; r < 4; ++r) {
        ep0[r] = __builtin_amdgcn_exp2f(p0[r] - mx); ep1[r] = pad ? 0.f : __builtin_amdgcn_exp2f(p1[r] - mx);
        en0[r] = __builtin_amdgcn_exp2f(mn - p0[r]); en1[r] = pad ? 0.f : __builtin_amdgcn_exp2f(mn - p1[r]);
        sp += ep0[r] + ep1[r]; sn += en0[r] + en1[r];
      }
      const float ip = __builtin_amdgcn_rcpf(xg_sum(sp)), in_ = __builtin_amdgcn_rcpf(xg_sum(sn));
      const v4u bp = pack8(ep0 * ip, ep1 * ip), bn = pack8(en0 * in_, en1 * in_);
      f32x4 n0 = mfma32(kvf[0], bp, Z4), n1 = mfma32(kvf[1], bp, Z4);
      n0 = mfma32(kvf[2], bn, n0); n1 = mfma32(kvf[3], bn, n1);
      const float den = xg_sum(pad ? n1[0] : 0.f) + 1e-6f;
      const float id = 1.f / den;
      if (pad) n1 = Z4;
      *(v4u*)(ob_l + (h * 2 + tt) * 1024 + lane * 16) = pack8(n0 * id, n1 * id);
    }
  }
  const int wv = wave < 7 ? wave : 6;
  const unsigned wo = (unsigned)OFF_WOF + (unsigned)(wv * 4) * 1024u;
  f32x4 acc[4][2];
#pragma unroll
  for (int n = 0; n < 4; ++n) { acc[n][0] = Z4; acc[n][1] = Z4; }
#pragma unroll
  for (int hb = 0; hb < 2; ++hb) {
    v4u Wo_[8][4];
    asm volatile("" ::: "memory");
#pragma unroll
    for (int h8 = 0; h8 < 8; ++h8)
#pragma unroll
      for (int n = 0; n < 4; ++n) Wo_[h8][n] = ld_s(rw, lane * 16u, wo + (unsigned)((hb * 8 + h8) * CT + n) * 1024u);
    if (hb == 0) BARRIER();
#pragma unroll
    for (int h8 = 0; h8 < 8; ++h8) {
      const int h = hb * 8 + h8;
      const v4u o0 = *(const v4u*)(ob_l + (h * 2) * 1024 + lane * 16), o1 = *(const v4u*)(ob_l + (h * 2 + 1) * 1024 + lane * 16);
#pragma unroll
      for (int n = 0; n < 4; ++n) { acc[n][0] = mfma32(Wo_[h8][n], o0, acc[n][0]); acc[n][1] = mfma32(Wo_[h8][n], o1, acc[n][1]); }
    }
#pragma unroll
    for (int n = 0; n < 4; ++n) PIN2(acc[n][0], acc[n][1]);
  }
  if (wave < 7) {
#pragma unroll
    for (int tt = 0; tt < 2; ++tt)
#pragma unroll
      for (int np = 0; np < 2; ++np)
        *(v4u*)(in.out + (size_t)(u * 32 + tt * 16 + rho) * 896 + (wave * 2 + np) * 32 + 8 * g) = pack8(acc[2 * np][tt], acc[2 * np + 1][tt]);
  }
}

DEV void parta_all(const In& in, unsigned char* wb, unsigned char* lds, int tid) {
  for (;;) { const int u = ticket(wb, CT_Q1, lds, tid); if (u >= 128) break; parta_unit1(in, wb, u, lds, tid); }
  for (;;) { const int u = ticket(wb, CT_Q2, lds, tid); if (u >= 64) break; parta_unit2(in, wb, u, lds, tid); }
}

DEV void unit_XS(unsigned char* wb, int s, int hq, unsigned char* lds, int tid) {
  const int lane = tid & 63, wave = rfl(tid >> 6);
  const int rho = lane & 15, g = lane >> 4;
  const int h = hq * 4 + (wave >> 1), tt = wave & 1;
  const rsrc_t rw = mk_rsrc(wb, (unsigned)OFF_DYN);
  const rsrc_t rd = mk_rsrc(wb + OFF_DYN, DYN_BYTES), rc = mk_rsrc(wb + OFF_CTRL, CTRL_BYTES);
  int budget = SPIN_BUDGET;
  const int tok = sig(tt, rho);
  v4u X[KS];
#pragma unroll
  for (int k = 0; k < KS; ++k) X[k] = ld_s(rw, lane * 16u, (unsigned)OFF_XFR + (unsigned)((s * 2 + tt) * KS + k) * 1024u);
  if (wave == 0) wait_words(rc, lane < 14 ? (unsigned)(CT_PROG + (hq * 14 + lane) * 128) : OOB, s + 1, budget, true);
  BARRIER();
  const unsigned gp = DY_GT + (unsigned)s * SEGB + (unsigned)(h * 2 * KS) * 1024u;
  f32x4 sx0 = Z4, sx1 = Z4, sm = Z4;
  {
    v4u G0[KS], G1[KS];
#pragma unroll
    for (int k = 0; k < KS; ++k) { G0[k] = ld_sc1s(rd, lane * 16u, gp + k * 1024); G1[k] = ld_sc1s(rd, lane * 16u, gp + (KS + k) * 1024); }
#pragma unroll
    for (int k = 0; k < KS; ++k) { sx0 = mfma32(G0[k], X[k], sx0); sx1 = mfma32(G1[k], X[k], sx1); }
    PIN2(sx0, sx1);
  }
  if (wave == 0) {
    wait_words(rc, lane == 0 ? (unsigned)CT_STEP : OOB, s < NSEG - 2 ? s + 2 : NSEG - 2, budget, true);
  }
  BARRIER();
  {
    const int key = ((rho & 7) >> 1) + 4 * (rho & 1);
    const unsigned rl = (unsigned)(g * 8 + key) * 16u;
    if (s >= NSEG - 2) poll1(rd, lane < KS ? DY_RC + (unsigned)((s * NH + h) * KS + lane) * 512u : OOB, budget);
    v4u R[KS];
    bool bad;
#pragma nounroll
    do {
      asm volatile("" ::: "memory");
      unsigned mm = 0;
#pragma unroll
      for (int k = 0; k < KS; ++k) R[k] = ld_sc1s(rd, rl, DY_RC + (unsigned)((s * NH + h) * KS + k) * 512u);
#pragma unroll
      for (int k = 0; k < KS; ++k) mm = max(mm, max(max(R[k].x, R[k].y), max(R[k].z, R[k].w)));
      bad = __any(mm == 0xFFFFFFFFu);
      budget -= 4;
    } while (bad && budget > 0);
#pragma unroll
    for (int k = 0; k < KS; ++k) sm = mfma32(R[k], X[k], sm);
    if (s == 63) STAMPG(hq, 14);
  }
  float v[8], w[4];
  float m = -3.0e38f;
#pragma unroll
  for (int j = 0; j < 8; ++j) { v[j] = (8 * g + j <= tok) ? (j < 4 ? sx0[j & 3] : sx1[j & 3]) : -3.0e38f; m = vmax(m, v[j]); }
#pragma unroll
  for (int j = 0; j < 4; ++j) { w[j] = g < 2 ? sm[j] : -3.0e38f; m = vmax(m, w[j]); }
  m = xg_max(m);
  float l = 0.f;
#pragma unroll
  for (int j = 0; j < 8; ++j) { v[j] = (8 * g + j <= tok) ? __builtin_amdgcn_exp2f(v[j] - m) : 0.f; l += v[j]; }
#pragma unroll
  for (int j = 0; j < 4; ++j) { w[j] = g < 2 ? __builtin_amdgcn_exp2f(w[j] - m) : 0.f; l += w[j]; }
  l = xg_sum(l);
  const float il = __builtin_amdgcn_rcpf(l);
  const v4u px = {pk2(v[0] * il, v[1] * il), pk2(v[2] * il, v[3] * il), pk2(v[4] * il, v[5] * il), pk2(v[6] * il, v[7] * il)};
  const v2u pm = {min(pk2(w[0] * il, w[1] * il), 0xFFFFFFFEu), min(pk2(w[2] * il, w[3] * il), 0xFFFFFFFEu)};
  st_sc1(rd, DY_PX + (unsigned)((s * NH + h) * 2 + tt) * 1024u + lane * 16u, nosentu(px));
  __builtin_amdgcn_raw_buffer_store_b64(pm, rd, g < 2 ? DY_PM + (unsigned)((s * 4 + hq) * 2 + tt) * 1024u + (unsigned)((h & 3) * 16 + rho) * 16u + 8u * g : OOB, 0, 16);
  if (s == 63) STAMPG(hq, 15);
  DRAIN_BARRIER();
  if (tid == 0) __hip_atomic_fetch_add((GAS unsigned*)(wb + OFF_CTRL + CT_XCNT + s * 128), 1u, __ATOMIC_RELAXED, __HIP_MEMORY_SCOPE_AGENT);
}

DEV void unit_XO(const In& in, unsigned char* wb, int s, int qt, unsigned char* lds, int tid) {
  const int lane = tid & 63, wave = rfl(tid >> 6);
  const int rho = lane & 15, g = lane >> 4;
  const rsrc_t rd = mk_rsrc(wb + OFF_DYN, DYN_BYTES), rc = mk_rsrc(wb + OFF_CTRL, CTRL_BYTES);
  int budget = SPIN_BUDGET;
  const int ct = qt * 7 + (wave < 7 ? wave : 6);
  if (wave == 0) wait_words(rc, lane < 56 ? (unsigned)(CT_PROG + (56 + lane) * 128) : OOB, s + 1, budget, true);
  BARRIER();
  v4u U[NH];
#pragma unroll
  for (int h = 0; h < NH; ++h) U[h] = ld_sc1s(rd, lane * 16u, DY_UT + (unsigned)s * SEGB + (unsigned)(h * CT + ct) * 1024u);
  if (wave == 0) {
    wait_words(rc, lane == 0 ? (unsigned)CT_STEP : OOB, s < NSEG - 2 ? s + 2 : NSEG - 2, budget, true);
  }
  BARRIER();
  v4u V[4];
  {
    bool bad;
#pragma nounroll
    do {
      asm volatile("" ::: "memory");
      unsigned mm = 0;
#pragma unroll
      for (int hq = 0; hq < 4; ++hq) V[hq] = ld_sc1s(rd, lane * 16u, DY_VC + (unsigned)((s * 4 + hq) * CT + ct) * 1024u);
#pragma unroll
      for (int hq = 0; hq < 4; ++hq) mm = max(mm, max(max(V[hq].x, V[hq].y), max(V[hq].z, V[hq].w)));
      bad = __any(mm == 0xFFFFFFFFu);
      budget -= 2;
    } while (bad && budget > 0);
  }
  if (s == 63) STAMPG(qt, 16);
  if (wave == 0 && s < NSEG - 2) wait_words(rc, lane == 0 ? (unsigned)(CT_XCNT + s * 128) : OOB, 4, budget, true);
  BARRIER();
  {
    if (s >= NSEG - 2) poll1(rd, lane < 32 ? DY_PX + (unsigned)s * 32768u + lane * 1024u : OOB, budget);
    v4u t[5];
    bool bad;
#pragma nounroll
    do {
      asm volatile("" ::: "memory");
      unsigned mm = 0;
#pragma unroll
      for (int i = 0; i < 4; ++i) t[i] = ld_sc1s(rd, tid * 16u, DY_PX + (unsigned)s * 32768u + i * 8192u);
      t[4] = ld_sc1s(rd, tid * 16u, DY_PM + (unsigned)s * 8192u);
#pragma unroll
      for (int i = 0; i < 5; ++i) mm = max(mm, max(max(t[i].x, t[i].y), max(t[i].z, t[i].w)));
      bad = __any(mm == 0xFFFFFFFFu);
      budget -= 2;
    } while (bad && budget > 0);
#pragma unroll
    for (int i = 0; i < 5; ++i) *(v4u*)(lds + (i * 512 + tid) * 16) = t[i];
  }
  BARRIER();
  if (s == 63) STAMPG(qt, 17);
  f32x4 a0 = Z4, a1 = Z4;
#pragma unroll
  for (int h = 0; h < NH; ++h) {
    a0 = mfma32(U[h], *(const v4u*)(lds + (h * 2) * 1024 + lane * 16), a0);
    a1 = mfma32(U[h], *(const v4u*)(lds + (h * 2 + 1) * 1024 + lane * 16), a1);
  }
#pragma unroll
  for (int hq = 0; hq < 4; ++hq) {
    a0 = mfma32(V[hq], *(const v4u*)(lds + 32768 + (hq * 2) * 1024 + lane * 16), a0);
    a1 = mfma32(V[hq], *(const v4u*)(lds + 32768 + (hq * 2 + 1) * 1024 + lane * 16), a1);
  }
  if (wave < 7) {
    v2u o0 = {pk2(a0[0], a0[1]), pk2(a0[2], a0[3])}, o1 = {pk2(a1[0], a1[1]), pk2(a1[2], a1[3])};
    *(v2u*)(in.out + (size_t)(s * 32 + sig(0, rho)) * 896 + 448 + ct * 16 + 4 * g) = o0;
    *(v2u*)(in.out + (size_t)(s * 32 + sig(1, rho)) * 896 + 448 + ct * 16 + 4 * g) = o1;
  }
}

DEV int get_slot(unsigned char* wb, unsigned char* lds, int tid) {
  int* sl = (int*)lds;
  if (tid == 0) {
    GAS unsigned* tick = (GAS unsigned*)(wb + OFF_CTRL + CT_TICK);
    const unsigned x = xcc_id() & 7u;
    const unsigned r = __hip_atomic_fetch_add(tick + x * 64, 1u, __ATOMIC_RELAXED, __HIP_MEMORY_SCOPE_AGENT);
    int slot;
    if (r < 32) slot = (int)(x * 32 + r);
    else {
      unsigned c[8]; int spins = 0;
      for (;;) {
        unsigned tot = 0;
#pragma unroll
        for (int k = 0; k < 8; ++k) { c[k] = __hip_atomic_load(tick + k * 64, __ATOMIC_RELAXED, __HIP_MEMORY_SCOPE_AGENT); tot += c[k]; }
        if (tot >= 256u || ++spins > SPIN_BUDGET) break;
      }
      unsigned idx = r - 32;
#pragma unroll
      for (int k = 0; k < 8; ++k) idx += ((unsigned)k < x && c[k] > 32) ? c[k] - 32 : 0;
      slot = -1;
#pragma unroll
      for (int k = 0; k < 8; ++k) {
        const unsigned fr = c[k] < 32 ? 32 - c[k] : 0;
        if (slot < 0) { if (idx < fr) slot = (int)(k * 32 + c[k] + idx); else idx -= fr; }
      }
    }
    sl[0] = slot;
  }
  __syncthreads();
  const int slot = rfl(sl[0]);
  __syncthreads();
  return slot;
}

constexpr int MAIN_LDS = 114688;
__global__ void __launch_bounds__(512, 2) k_main(In in) {
  extern __shared__ __attribute__((aligned(16))) unsigned char lds[];
  unsigned char* wb = ws_base(in.ws);
  const int tid = threadIdx.x;
  const int slot = get_slot(wb, lds, tid);
  if (slot < 0) return;
  STAMPG(slot, 0);
  const int idx = (slot & 31) * 4 + ((slot >> 5) & 3);
  if (slot < 128) {
    if (idx < 64) {
      const int h = idx >> 2, q = idx & 3;
      if (q < 2) role_S<true>(wb, h, q, lds, tid); else role_S<false>(wb, h, q, lds, tid);
    } else if (idx < 120) {
      const int o = idx - 64;
      role_O(wb, (o >> 2) & 1, (o >> 3) * 4 + (o & 3), lds, tid);
    }
  } else {
    if (idx < 56) gu_worker<true>(wb, idx, lds, tid);
    else if (idx < 112) gu_worker<false>(wb, idx, lds, tid);
    else for (int i = 0; i < MDELAY; ++i) __builtin_amdgcn_s_sleep(16);
    parta_all(in, wb, lds, tid);
    STAMPG(slot, 2);
    for (;;) {
      const int u = ticket(wb, CT_Q3, lds, tid);
      if (u >= NSEG * 8) break;
      if (u & 4) unit_XO(in, wb, u >> 3, u & 3, lds, tid); else unit_XS(wb, u >> 3, u & 3, lds, tid);
      if (u >= NSEG * 8 - 16) break;
    }
  }
  STAMPG(slot, 3);
}

extern "C" void kernel_launch(void* const* d_in, const int* in_sizes, int n_in,
                              void* d_out, int out_size, void* d_ws, size_t ws_size,
                              hipStream_t stream) {
  In in;
  in.Ax = (const u16*)d_in[0]; in.Awqkv = (const u16*)d_in[1]; in.Awphi = (const u16*)d_in[2]; in.Awo = (const u16*)d_in[3];
  in.Bx = (const u16*)d_in[4]; in.Bmem0 = (const u16*)d_in[5]; in.Wq = (const u16*)d_in[6]; in.Wk = (const u16*)d_in[7];
  in.Wv = (const u16*)d_in[8]; in.Wo = (const u16*)d_in[9];
  in.out = (u16*)d_out; in.ws = d_ws;
  (void)hipFuncSetAttribute((const void*)k_prep, hipFuncAttributeMaxDynamicSharedMemorySize, 57344);
  (void)hipFuncSetAttribute((const void*)k_main, hipFuncAttributeMaxDynamicSharedMemorySize, MAIN_LDS);
  k_prep<<<PREP_GRID, 448, 57344, stream>>>(in);
  k_main<<<256, 512, MAIN_LDS, stream>>>(in);
}
```

```cpp
#include <hip/hip_runtime.h>
#include <hip/hip_bf16.h>

typedef unsigned short u16;
typedef __bf16 bf16x8 __attribute__((ext_vector_type(8)));
typedef __bf16 bf16x2 __attribute__((ext_vector_type(2)));
typedef short s16x4 __attribute__((ext_vector_type(4)));
typedef float f32x4 __attribute__((ext_vector_type(4)));
typedef unsigned v4u __attribute__((ext_vector_type(4)));
typedef unsigned v2u __attribute__((ext_vector_type(2)));
typedef unsigned v8u __attribute__((ext_vector_type(8)));
typedef __bf16 bf16x16 __attribute__((ext_vector_type(16)));
#define DEV __device__ __forceinline__
#define LAS __attribute__((address_space(3)))
#define GAS __attribute__((address_space(1)))

constexpr int DM = 448, NH = 16, EH = 256, NSEG = 64, KS = 14, CT = 28;
constexpr size_t MB = 1u << 20;
constexpr size_t OFF_XCH = 0;
constexpr size_t XCH_BYTES = 6 * MB;
constexpr int XCH_PAIRS_USED = 316;
constexpr size_t OFF_CTRL = 6 * MB;
constexpr size_t OFF_AFR = 8 * MB;
constexpr size_t OFF_BTFR = 16 * MB;
constexpr size_t OFF_XFR = 24 * MB;
constexpr size_t OFF_WQF = 26 * MB;
constexpr size_t OFF_WKF = 27 * MB;
constexpr size_t OFF_WVF = 28 * MB;
constexpr size_t OFF_WOF = 29 * MB;
constexpr size_t OFF_XFR2 = 30 * MB;
constexpr size_t OFF_DYN = 32 * MB;
constexpr unsigned DY_GT = 0;
constexpr unsigned DY_UT = 32u << 20;
constexpr unsigned DY_RC = 64u << 20;
constexpr unsigned DY_VC = 72u << 20;
constexpr unsigned DY_PX = 80u << 20;
constexpr unsigned DY_PM = 84u << 20;
constexpr unsigned DY_KV = 86u << 20;
constexpr unsigned DY_KVP = 87u << 20;
constexpr unsigned DYN_BYTES = 88u << 20;
constexpr unsigned SEGB = NH * CT * 1024;
constexpr unsigned OOB = 0xFFFFFF00u;
constexpr int SPIN_BUDGET = 1 << 20;
constexpr int CT_VOTE = 0;
constexpr int CT_TICK = 4096;
constexpr int CT_ABORT = 8192;
constexpr int CT_STEP = 8448;
constexpr int CT_ACNT = 8704;
constexpr int CT_Q1 = 8960;
constexpr int CT_Q2 = 9216;
constexpr int CT_HCNT = 10240;
constexpr int CT_PROG = 16384;
constexpr int CT_Q3 = 9472;
constexpr int CT_DONE = 12544;
constexpr int CT_XCNT = 32768;
constexpr int CTRL_BYTES = 40960;

DEV unsigned char* ws_base(void* ws) { const size_t a = (size_t)ws; return (unsigned char*)ws + (((a + (2 * MB - 1)) & ~(size_t)(2 * MB - 1)) - a); }
DEV unsigned pk2(float a, float b) { bf16x2 v = {(__bf16)a, (__bf16)b}; return __builtin_bit_cast(unsigned, v); }
DEV f32x4 mfma32(v4u a, v4u b, f32x4 c) { return __builtin_amdgcn_mfma_f32_16x16x32_bf16(__builtin_bit_cast(bf16x8, a), __builtin_bit_cast(bf16x8, b), c, 0, 0, 0); }
DEV f32x4 smfma64(v4u a, v8u b, f32x4 c, int idx) { return __builtin_amdgcn_smfmac_f32_16x16x64_bf16(__builtin_bit_cast(bf16x8, a), __builtin_bit_cast(bf16x16, b), c, idx, 0, 0); }
DEV v8u cat8(v4u a, v4u b) { return __builtin_shufflevector(a, b, 0, 1, 2, 3, 4, 5, 6, 7); }
DEV v4u pack8(f32x4 a, f32x4 b) { v4u r; r.x = pk2(a[0], a[1]); r.y = pk2(a[2], a[3]); r.z = pk2(b[0], b[1]); r.w = pk2(b[2], b[3]); return r; }
DEV int sig(int tau, int rho) { return 8 * (rho >> 2) + 4 * tau + (rho & 3); }
DEV int sig2(int tau, int rho) { const int g = rho >> 2, r = rho & 3; return 16 * (g >> 1) + 8 * tau + 4 * (r >> 1) + 2 * (g & 1) + (r & 1); }
DEV unsigned xcc_id() { return (unsigned)__builtin_amdgcn_s_getreg(20 | (3 << 11)) & 0xfu; }
DEV int rfl(int x) { return __builtin_amdgcn_readfirstlane(x); }
DEV float f_as(unsigned x) { return __builtin_bit_cast(float, x); }
DEV unsigned u_as(float x) { return __builtin_bit_cast(unsigned, x); }
template <int CTRL> DEV float dppf(float x) { return f_as((unsigned)__builtin_amdgcn_update_dpp(0, (int)u_as(x), CTRL, 0xf, 0xf, true)); }
template <int CTRL> DEV unsigned dppu(unsigned x) { return (unsigned)__builtin_amdgcn_update_dpp(0, (int)x, CTRL, 0xf, 0xf, true); }
constexpr int DPP_ROR8 = 0x128, DPP_ROR4 = 0x124, DPP_XOR1 = 0xB1, DPP_XOR2 = 0x4E;
DEV float vmax(float a, float b) { return __builtin_elementwise_maximum(a, b); }
DEV float xg_max(float m) {
  auto a = __builtin_amdgcn_permlane32_swap(u_as(m), u_as(m), false, false);
  m = vmax(f_as(a[0]), f_as(a[1]));
  auto b = __builtin_amdgcn_permlane16_swap(u_as(m), u_as(m), false, false);
  return vmax(f_as(b[0]), f_as(b[1]));
}
DEV float xg_sum(float m) {
  auto a = __builtin_amdgcn_permlane32_swap(u_as(m), u_as(m), false, false);
  m = f_as(a[0]) + f_as(a[1]);
  auto b = __builtin_amdgcn_permlane16_swap(u_as(m), u_as(m), false, false);
  return f_as(b[0]) + f_as(b[1]);
}
typedef __amdgpu_buffer_rsrc_t rsrc_t;
DEV rsrc_t mk_rsrc(void* p, unsigned bytes) { return __builtin_amdgcn_make_buffer_rsrc(p, 0, (int)bytes, 0x00020000); }
DEV v4u ld_sc1(rsrc_t r, unsigned off) { return __builtin_amdgcn_raw_buffer_load_b128(r, off, 0, 16); }
DEV v4u ld_sc1s(rsrc_t r, unsigned voff, unsigned soff) { return __builtin_amdgcn_raw_buffer_load_b128(r, voff, soff, 16); }
DEV v4u ld_s(rsrc_t r, unsigned voff, unsigned soff) { return __builtin_amdgcn_raw_buffer_load_b128(r, voff, soff, 0); }
DEV void lds_dma(rsrc_t r, unsigned char* ldsp, unsigned voff, unsigned soff) { __builtin_amdgcn_raw_ptr_buffer_load_lds(r, (LAS void*)ldsp, 16, voff, soff, 0, 0); }
DEV void st_sc1(rsrc_t r, unsigned off, v4u v) { __builtin_amdgcn_raw_buffer_store_b128(v, r, off, 0, 16); }
DEV unsigned ld32_sc1(rsrc_t r, unsigned off) { return __builtin_amdgcn_raw_buffer_load_b32(r, off, 0, 16); }
DEV void st32_sc1(rsrc_t r, unsigned off, unsigned v) { __builtin_amdgcn_raw_buffer_store_b32(v, r, off, 0, 16); }
DEV unsigned long long memtime() { unsigned long long t; asm volatile("s_memtime %0\n\ts_waitcnt lgkmcnt(0)" : "=s"(t) :: "memory"); return t; }
#define GUDELAY 24
#define LEAD 5
#define ODELAY 0
#define MDELAY 12
#define TA1 1230u
#define TA2 650u
#define TA3 2800u
#define TDX 400u
#define TUNE_LOAD
DEV unsigned now32() { return (unsigned)__builtin_amdgcn_s_memtime(); }
DEV void wait_abs(unsigned t) { while ((int)(now32() - t) < 0) {} }
DEV v4u nosentu(v4u r) { v4u o = {min(r.x, 0xFFFFFFFEu), min(r.y, 0xFFFFFFFEu), min(r.z, 0xFFFFFFFEu), min(r.w, 0xFFFFFFFEu)}; return o; }
DEV v4u nosent(f32x4 r) { v4u o = {min(u_as(r[0]), 0xFFFFFFFEu), min(u_as(r[1]), 0xFFFFFFFEu), min(u_as(r[2]), 0xFFFFFFFEu), min(u_as(r[3]), 0xFFFFFFFEu)}; return o; }
#define BARRIER() asm volatile("s_waitcnt lgkmcnt(0)\n\ts_barrier" ::: "memory")
#define DRAIN_BARRIER() asm volatile("s_waitcnt vmcnt(0) lgkmcnt(0)\n\ts_barrier" ::: "memory")
#define Z4 ((f32x4){0.f, 0.f, 0.f, 0.f})
#define PIN2(a, b) asm volatile("" : "+v"(a), "+v"(b) :: "memory")

#define STAMPV(role, s, wave, k) do {} while (0)
#define STAMPDUMP(role) do {} while (0)
#define STAMPX(s, wave, k, val) do {} while (0)
#define STAMPG(slot, k) do {} while (0)
#define STAMPC(slot, k) do {} while (0)
#define STAMPP(k) do {} while (0)
#define STAMPW(slot, k) do {} while (0)

#define CP_DECL
#define CP(k) do {} while (0)
#define CP_DUMP(role) do {} while (0)
#define CPV(x) do {} while (0)
struct In {
  const u16 *Ax, *Awqkv, *Awphi, *Awo, *Bx, *Bmem0, *Wq, *Wk, *Wv, *Wo;
  u16* out; void* ws;
};

DEV v4u ld16_a8(const u16* p) { v2u a = *(const v2u*)p, b = *(const v2u*)(p + 4); return (v4u){a.x, a.y, b.x, b.y}; }

DEV void prep_parta_unit(const In& in, unsigned char* wb, int u, int lane) {
  const int rho = lane & 15, g = lane >> 4;
  if (u < 3 * NH * KS) {
    const int ks = u % KS, h = (u / KS) % NH, which = u / (KS * NH);
    v4u a[2], b[2];
#pragma unroll
    for (int t = 0; t < 2; ++t) {
      const int k = 32 * ks + sig(t, rho);
      const u16* src = in.Awqkv + (size_t)k * 1344 + which * 448 + h * 28 + 8 * g;
      v2u lo = *(const v2u*)src, hi = {0u, 0u};
      if (g < 3) hi = *(const v2u*)(src + 4);
      a[t] = (v4u){lo.x, lo.y, hi.x, hi.y};
    }
#pragma unroll
    for (int t = 0; t < 2; ++t) {
      const int f = sig(t, rho);
      unsigned e[8];
#pragma unroll
      for (int j = 0; j < 8; ++j) {
        const int d = 8 * g + j;
        unsigned v = 0;
        if (d < 28 && f < 28) v = which == 2 ? (d == f ? 0x3F80u : 0u) : (unsigned)in.Awphi[(h * 28 + d) * 28 + f];
        e[j] = v;
      }
      b[t] = (v4u){e[0] | (e[1] << 16), e[2] | (e[3] << 16), e[4] | (e[5] << 16), e[6] | (e[7] << 16)};
    }
    const float sc = which == 2 ? 1.f : 1.4426950408889634f;
    unsigned char* dst = wb + (which == 0 ? OFF_WQF : (which == 1 ? OFF_WKF : OFF_WVF));
#pragma unroll
    for (int t = 0; t < 2; ++t) {
      f32x4 d0 = mfma32(a[0], b[t], Z4), d1 = mfma32(a[1], b[t], Z4);
      *(v4u*)(dst + ((size_t)(h * 2 + t) * KS + ks) * 1024 + lane * 16) = pack8(d0 * sc, d1 * sc);
    }
  } else {
    const int v = u - 3 * NH * KS, np = v % KS, h = v / KS;
    v4u a[2];
#pragma unroll
    for (int t = 0; t < 2; ++t) {
      const int d = sig(t, rho);
      a[t] = (v4u){0u, 0u, 0u, 0u};
      if (d < 28) a[t] = *(const v4u*)(in.Awo + (size_t)(h * 28 + d) * DM + np * 32 + 8 * g);
    }
#pragma unroll
    for (int t = 0; t < 2; ++t) {
      const int n = sig(t, rho) - 8 * g;
      unsigned e[4] = {0u, 0u, 0u, 0u};
#pragma unroll
      for (int j = 0; j < 4; ++j) e[j] = (n == 2 * j ? 0x3F80u : 0u) | (n == 2 * j + 1 ? 0x3F800000u : 0u);
      const v4u b = {e[0], e[1], e[2], e[3]};
      f32x4 d0 = mfma32(a[0], b, Z4), d1 = mfma32(a[1], b, Z4);
      *(v4u*)(wb + OFF_WOF + (size_t)(h * CT + np * 2 + t) * 1024 + lane * 16) = pack8(d0, d1);
    }
  }
}

#define PAUX 16
#define PST(r, off, v) __builtin_amdgcn_raw_buffer_store_b128(v, r, off, 0, PAUX)
constexpr unsigned NFILL = 88;
constexpr int PREP_GRID = 256 + 128 + 37 + NFILL;
__global__ void __launch_bounds__(448, 4) k_prep(In in) {
  extern __shared__ __attribute__((aligned(16))) unsigned char lds[];
  unsigned char* wb = ws_base(in.ws);
  const int tid = threadIdx.x, lane = tid & 63, wave = rfl(tid >> 6), b = blockIdx.x;
  const int rho = lane & 15, g = lane >> 4;
  STAMPP(0);
  if (b >= 421) {
    const rsrc_t rw = mk_rsrc(wb, (unsigned)OFF_DYN), rd = mk_rsrc(wb + OFF_DYN, DYN_BYTES);
    const unsigned nt16 = NFILL * 448u * 16u, t0 = (unsigned)((b - 421) * 448 + tid);
    for (unsigned o = t0; o < (CTRL_BYTES - CT_TICK) / 4; o += NFILL * 448u) *(unsigned*)(wb + OFF_CTRL + CT_TICK + o * 4) = 0u;
    const v4u ones = {~0u, ~0u, ~0u, ~0u};
    for (unsigned o = t0 * 16u; o < XCH_PAIRS_USED * 16384u; o += nt16) {
      v4u v = ones;
      if (o < 16384) {
        const int w = (int)(o & 8191);
        if (w < 7168) {
          const int slot = w >> 7, cg = slot >> 1, rg = slot & 1, ii = (w >> 5) & 3, hf = (w >> 4) & 1;
          const unsigned* mp = (const unsigned*)(in.Bmem0 + (rg * 4 + ii) * DM + cg * 16) + hf;
          v = nosentu((v4u){mp[0], mp[2], mp[4], mp[6]});
        }
      }
      PST(rw, (unsigned)OFF_XCH + o, v);
    }
    const unsigned RB = NH * KS * 512u;
    for (unsigned o = t0 * 16u; o < 2u * SEGB; o += nt16) { PST(rd, DY_GT + o, ones); PST(rd, DY_UT + o, ones); }
    for (unsigned o = t0 * 16u; o < 2u * RB; o += nt16) { PST(rd, DY_RC + 62u * RB + o, ones); PST(rd, DY_VC + 62u * RB + o, ones); }
    for (unsigned o = t0 * 16u; o < 65536u; o += nt16) PST(rd, DY_PX + 62u * 32768u + o, ones);
    for (unsigned o = t0 * 16u; o < 16384u; o += nt16) PST(rd, DY_PM + 62u * 8192u + o, ones);
    STAMPP(3);
    return;
  }
  if (b >= 384) {
    const int pi = (b - 384) * 7 + wave;
    if (lane == 0 && pi < 256) {
      const int pair = (pi * 3) >> 1;
      unsigned* p0 = (unsigned*)(wb + OFF_XCH + (size_t)pair * 16384 + 8064);
      unsigned* p1 = p0 + 2048;
      unsigned t0 = ~0u, t1 = ~0u;
      for (int rep = 0; rep < 3; ++rep) {
        unsigned v, v2, z = 0;
        unsigned long long a = memtime();
        asm volatile("global_atomic_or %0, %1, %2, off sc0\n\ts_waitcnt vmcnt(0)" : "=v"(v) : "v"(p0), "v"(z) : "memory");
        unsigned long long bb = memtime();
        asm volatile("global_atomic_or %0, %1, %2, off sc0\n\ts_waitcnt vmcnt(0)" : "=v"(v2) : "v"(p1), "v"(z) : "memory");
        unsigned long long c = memtime();
        t0 = min(t0, (unsigned)(bb - a)); t1 = min(t1, (unsigned)(c - bb));
      }
      const int vote = (t1 < t0) ^ (xcc_id() >= 4);
      const unsigned d = t0 > t1 ? t0 - t1 : t1 - t0;
      ((int*)(wb + OFF_CTRL + CT_VOTE))[pi] = (pair >> 7) * 4 + (d > 100 ? vote : 2);
    }
    STAMPP(3);
    return;
  }
  if (b >= 256) { prep_parta_unit(in, wb, (b - 256) * 7 + wave, lane); STAMPP(3); return; }
  const int mat = (b & 7) * 4 + (b >> 6), sub = (b >> 3) & 7;
  const int m = mat >> 4, h = mat & 15, rh = sub >> 2, cq = sub & 3;
  const rsrc_t rw = mk_rsrc(wb, (unsigned)OFF_DYN);
  const rsrc_t rq = mk_rsrc((void*)(m == 0 ? in.Wq : in.Wo), 448u * 4096u * 2u);
  const rsrc_t rp_ = mk_rsrc((void*)(m == 0 ? in.Wk : in.Wv), 448u * 4096u * 2u);
  const int rp = rh * 7 + wave;
  unsigned qv[8];
  if (m == 0) {
    const int T = cq * 7 + wave, k = 32 * (T >> 1) + sig2(T & 1, rho);
#pragma unroll
    for (int es = 0; es < 8; ++es) qv[es] = (unsigned)(k * 4096 + h * EH + es * 32 + g * 8) * 2u;
  } else {
#pragma unroll
    for (int i = 0; i < 8; ++i) {
      const int n = ((i >> 2) * 28 + wave * 4 + (i & 3)) * 64 + lane, e = n / 14, ch = n % 14;
      qv[i] = (unsigned)((h * EH + e) * DM + cq * 112 + ch * 8) * 2u;
    }
  }
  const unsigned pv0 = (unsigned)((32 * rp + sig(0, rho)) * 4096 + h * EH + g * 8) * 2u, pv1 = pv0 + 4u * 4096u * 2u;
  v4u pa[2][8];
#pragma unroll
  for (int hf = 0; hf < 2; ++hf) {
#pragma unroll
    for (int i = 0; i < 4; ++i) {
      const int slot = m == 0 ? wave * 8 + hf * 4 + i : hf * 28 + wave * 4 + i;
      lds_dma(rq, lds + slot * 1024, qv[hf * 4 + i], 0u);
    }
#pragma unroll
    for (int i = 0; i < 4; ++i) { pa[0][hf * 4 + i] = ld_s(rp_, pv0, (hf * 4 + i) * 64u); pa[1][hf * 4 + i] = ld_s(rp_, pv1, (hf * 4 + i) * 64u); }
  }
  v4u xv;
  {
    const int j = b * 448 + tid, l = j & 63, f = j >> 6, cs = f % KS, tau = (f / KS) & 1, s = f / (2 * KS);
    xv = *(const v4u*)(in.Bx + (size_t)(s * 32 + sig(tau, l & 15)) * DM + cs * 32 + (l >> 4) * 8);
  }
  {
    const float sc = m ? 1.f : 0.0625f * 1.4426950408889634f;
    const unsigned dst = (unsigned)(m ? OFF_BTFR : OFF_AFR) + (unsigned)((h * CT + cq * 7) * KS + rp) * 1024u + lane * 16u;
    const unsigned troff = (unsigned)((8 * g + (rho >> 2)) * 224 + (rho & 3) * 8);
    DRAIN_BARRIER();
    STAMPP(1);
    if (m == 0) {
      v4u qb[2][8];
#pragma unroll
      for (int es = 0; es < 8; ++es) qb[0][es] = *(const v4u*)(lds + es * 1024 + lane * 16);
#pragma unroll
      for (int i = 0; i < 7; ++i) {
        f32x4 a0 = Z4, a1 = Z4;
#pragma unroll
        for (int es = 0; es < 8; ++es) {
          if (i + 1 < 7 && (es & 1) == 0) {
            qb[(i + 1) & 1][es] = *(const v4u*)(lds + ((i + 1) * 8 + es) * 1024 + lane * 16);
          }
          a0 = mfma32(pa[0][es], qb[i & 1][es], a0); a1 = mfma32(pa[1][es], qb[i & 1][es], a1);
          if (i + 1 < 7 && (es & 1) == 1) {
            qb[(i + 1) & 1][es] = *(const v4u*)(lds + ((i + 1) * 8 + es) * 1024 + lane * 16);
          }
        }
        PST(rw, dst + i * (KS * 1024u), pack8(a0 * sc, a1 * sc));
      }
    } else {
#pragma unroll
      for (int i = 0; i < 7; ++i) {
        f32x4 a0 = Z4, a1 = Z4;
#pragma unroll
        for (int es = 0; es < 8; ++es) {
          s16x4 lo = __builtin_amdgcn_ds_read_tr16_b64_v4i16((LAS s16x4*)(lds + troff + es * 32 * 224 + i * 32));
          s16x4 hi = __builtin_amdgcn_ds_read_tr16_b64_v4i16((LAS s16x4*)(lds + troff + (es * 32 + 4) * 224 + i * 32));
          v2u l2 = __builtin_bit_cast(v2u, lo), h2 = __builtin_bit_cast(v2u, hi);
          const v4u qb = {l2.x, l2.y, h2.x, h2.y};
          a0 = mfma32(pa[0][es], qb, a0); a1 = mfma32(pa[1][es], qb, a1);
        }
        PST(rw, dst + i * (KS * 1024u), pack8(a0, a1));
      }
    }
  }
  STAMPP(2);
  PST(rw, (unsigned)OFF_XFR + (unsigned)(b * 448 + tid) * 16u, xv);
  {
    auto e0 = __builtin_amdgcn_permlane16_swap(xv.x, xv.y, false, false);
    auto e1 = __builtin_amdgcn_permlane16_swap(xv.z, xv.w, false, false);
    const v4u x2 = {e0[0], e1[0], e0[1], e1[1]};
    PST(rw, (unsigned)OFF_XFR2 + (unsigned)(b * 448 + tid) * 16u, x2);
  }
  STAMPP(3);
}

DEV unsigned read_parity(const unsigned char* wb, int lane) {
  const int* v = (const int*)(wb + OFF_CTRL + CT_VOTE);
  int x[4];
#pragma unroll
  for (int k = 0; k < 4; ++k) x[k] = v[k * 64 + lane];
  unsigned par = 0;
#pragma unroll
  for (int p = 0; p < 3; ++p) {
    int n1 = 0, n0 = 0;
#pragma unroll
    for (int k = 0; k < 4; ++k) { n1 += __popcll(__ballot(x[k] == p * 4 + 1)); n0 += __popcll(__ballot(x[k] == p * 4)); }
    par |= (n1 > n0 ? 1u : 0u) << p;
  }
  return par;
}
DEV unsigned pair_off(unsigned par, int p) { return (unsigned)p * 16384u + (((par >> (p >> 7)) & 1u) << 13); }
DEV unsigned memx_off(unsigned par, int s) { return pair_off(par, s); }
DEV unsigned scx_off(unsigned par, int s, int rg, int h) { return pair_off(par, 64 + 4 * s + 2 * rg + (h >> 3)) + (h & 7) * 1024; }
DEV bool any_invalid(v4u v) { unsigned m = max(max(v.x, v.y), max(v.z, v.w)); return __any(m == 0xFFFFFFFFu); }
DEV v4u poll1(rsrc_t rx, unsigned off, int& budget) {
  v4u t;
#pragma nounroll
  do { asm volatile("" ::: "memory"); t = ld_sc1(rx, off); --budget; } while (any_invalid(t) && budget > 0);
  return t;
}
DEV void gather_chunk(rsrc_t rx, unsigned off, unsigned char* dst, int& budget) {
  v4u t = ld_sc1(rx, off);
  *(v4u*)dst = t;
  if (__builtin_expect(any_invalid(t), 0)) { t = poll1(rx, off, budget); *(v4u*)dst = t; }
}
DEV int min_lanes8(int v) {
  v = min(v, (int)dppu<DPP_XOR1>((unsigned)v)); v = min(v, (int)dppu<DPP_XOR2>((unsigned)v));
  return min(__builtin_amdgcn_readlane(v, 0), __builtin_amdgcn_readlane(v, 4));
}
DEV int wait_words(rsrc_t rc, unsigned off, int need, int& budget, bool sleep) {
  int mn;
  for (;;) {
    asm volatile("" ::: "memory");
    const int v = off == OOB ? 0x7fffffff : (int)ld32_sc1(rc, off);
    --budget;
    if (__all(v >= need) || budget <= 0) {
      mn = need;
      if (__all(v >= need + 16)) mn = need + 16; else if (__all(v >= need + 4)) mn = need + 4;
      break;
    }
    if (sleep) { __builtin_amdgcn_s_sleep(8); budget -= 4; }
  }
  return mn;
}

template <bool ISG>
DEV void gu_compute(const v4u (&W)[2][KS], const unsigned char* xb, v4u& r0, v4u& r1) {
  f32x4 a00 = Z4, a01 = Z4, a10 = Z4, a11 = Z4;
  v4u x0[KS], x1[KS];
#pragma unroll
  for (int k = 0; k < 2; ++k) { x0[k] = *(const v4u*)(xb + k * 1024); x1[k] = *(const v4u*)(xb + (KS + k) * 1024); }
  __builtin_amdgcn_sched_barrier(0);
#pragma unroll
  for (int k = 0; k < KS; ++k) {
    if (k + 2 < KS) { x0[k + 2] = *(const v4u*)(xb + (k + 2) * 1024); x1[k + 2] = *(const v4u*)(xb + (KS + k + 2) * 1024); }
    if (ISG) {
      a00 = mfma32(W[0][k], x0[k], a00); a10 = mfma32(W[1][k], x0[k], a10);
      a01 = mfma32(W[0][k], x1[k], a01); a11 = mfma32(W[1][k], x1[k], a11);
    } else {
      a00 = mfma32(x0[k], W[0][k], a00); a10 = mfma32(x0[k], W[1][k], a10);
      a01 = mfma32(x1[k], W[0][k], a01); a11 = mfma32(x1[k], W[1][k], a11);
    }
    __builtin_amdgcn_sched_barrier(0);
  }
  if (ISG) { r0 = pack8(a00, a10); r1 = pack8(a01, a11); } else { r0 = pack8(a00, a01); r1 = pack8(a10, a11); }
}

template <bool ISG>
DEV void gu_worker(unsigned char* wb, int w, unsigned char* lds, int tid) {
  const int lane = tid & 63, wave = rfl(tid >> 6), tp = wave & 3, sp = wave >> 2;
  const unsigned wfr = (unsigned)(ISG ? OFF_AFR : OFF_BTFR);
  const rsrc_t rw = mk_rsrc(wb, (unsigned)OFF_DYN);
  const rsrc_t rd = mk_rsrc(wb + OFF_DYN, DYN_BYTES), rc = mk_rsrc(wb + OFF_CTRL, CTRL_BYTES);
  const int T = 8 * (ISG ? w : w - 56) + 2 * tp;
  const int h = T / CT, tl = T % CT;
  auto stage = [&](int s, int buf) {
#pragma unroll
    for (int i = 0; i < 7; ++i)
      lds_dma(rw, lds + buf * 57344 + i * 8192 + wave * 1024, tid * 16u, (unsigned)OFF_XFR + (unsigned)s * 28672u + i * 8192u);
  };
  stage(0, 0);
  __builtin_amdgcn_sched_barrier(0);
  const unsigned o0 = (ISG ? (unsigned)((h * 2 + 0) * KS + (tl >> 1)) : (unsigned)T) * 1024u + lane * 16u;
  const unsigned o1 = (ISG ? (unsigned)((h * 2 + 1) * KS + (tl >> 1)) : (unsigned)(T + 1)) * 1024u + lane * 16u;
  if (sp == 1) {
    asm volatile("s_waitcnt vmcnt(0)\n\ts_barrier" ::: "memory");
    __builtin_amdgcn_s_sleep(GUDELAY);
  }
  v4u W[2][KS];
#pragma unroll
  for (int k = 0; k < KS; ++k)
#pragma unroll
    for (int t = 0; t < 2; ++t) W[t][k] = ld_s(rw, lane * 16u, wfr + (unsigned)((T + t) * KS + k) * 1024u);
  __builtin_amdgcn_sched_barrier(0);
  STAMPG(128 + w, 8); STAMPC(128 + w, 9);
  if (sp == 0) {
    asm volatile("s_waitcnt vmcnt(28)\n\ts_barrier" ::: "memory");
#pragma nounroll
    for (int sg = 0; sg < 2; ++sg) {
      v4u r0, r1;
      gu_compute<ISG>(W, lds + sg * 28672 + lane * 16, r0, r1);
      const unsigned ob = (ISG ? DY_GT : DY_UT) + (unsigned)sg * SEGB;
      st_sc1(rd, ob + o0, nosentu(r0)); st_sc1(rd, ob + o1, nosentu(r1));
    }
    STAMPG(128 + w, 11);
  }
  stage(2, 1);
  int buf = 1;
  for (int s = 2; s < NSEG; s += 2) {
    if (s <= 4) asm volatile("s_waitcnt vmcnt(0) lgkmcnt(0)\n\ts_barrier" ::: "memory");
    else asm volatile("s_waitcnt vmcnt(2) lgkmcnt(0)\n\ts_barrier" ::: "memory");
    if (wave == 7) st32_sc1(rc, lane == 0 ? (unsigned)(CT_PROG + w * 128) : OOB, (unsigned)(s <= 4 ? s : s - 2));
    stage(s + 2 < NSEG ? s + 2 : s, buf ^ 1);
    v4u r0, r1;
    gu_compute<ISG>(W, lds + buf * 57344 + sp * 28672 + lane * 16, r0, r1);
    const unsigned ob = (ISG ? DY_GT : DY_UT) + (unsigned)(s + sp) * SEGB;
    st_sc1(rd, ob + o0, nosentu(r0)); st_sc1(rd, ob + o1, nosentu(r1));
    buf ^= 1;
  }
  DRAIN_BARRIER();
  if (wave == 7) st32_sc1(rc, lane == 0 ? (unsigned)(CT_PROG + w * 128) : OOB, (unsigned)NSEG);
  STAMPG(128 + w, 5); STAMPC(128 + w, 10);
}

template <bool HASG>
DEV void role_S(unsigned char* wb, int h, int q, unsigned char* lds, int tid) {
  const int lane = tid & 63, wave = rfl(tid >> 6);
  TUNE_LOAD
  unsigned char* rimg = lds;
  unsigned char* mimg = lds + 4096;
  unsigned char* simg = mimg;
  unsigned char* gring = lds + 20480;
  const rsrc_t rx = mk_rsrc(wb + OFF_XCH, (unsigned)XCH_BYTES);
  int budget = SPIN_BUDGET;
  if (HASG && wave >= 4) {
    const rsrc_t rd = mk_rsrc(wb + OFF_DYN, DYN_BYTES);
    const int f0 = (wave - 4) * 7;
    const unsigned src = DY_GT + (unsigned)((h * 2 + q) * KS + (f0 % KS)) * 1024u + (unsigned)(f0 / KS) * SEGB;
    v4u t[7];
    bool bad;
#pragma nounroll
    do {
      asm volatile("" ::: "memory");
      unsigned m = 0;
#pragma unroll
      for (int k = 0; k < 7; ++k) t[k] = ld_sc1s(rd, lane * 16u, src + k * 1024u);
#pragma unroll
      for (int k = 0; k < 7; ++k) m = max(m, max(max(t[k].x, t[k].y), max(t[k].z, t[k].w)));
      bad = __any(m == 0xFFFFFFFFu);
      budget -= 2;
    } while (bad && budget > 0);
#pragma unroll
    for (int k = 0; k < 7; ++k) *(v4u*)(gring + (f0 + k) * 1024 + lane * 16) = t[k];
    if (wave == 7) STAMPW(h * 4 + q, 12);
  }
  if (wave == 7) {
    if (!HASG) { BARRIER(); return; }
    const rsrc_t rd = mk_rsrc(wb + OFF_DYN, DYN_BYTES), rc = mk_rsrc(wb + OFF_CTRL, CTRL_BYTES);
    const int w0 = (28 * h) >> 3, w1 = (28 * h + 27) >> 3;
    const unsigned poff = (w0 + lane <= w1) ? (unsigned)(CT_PROG + (w0 + lane) * 128) : OOB;
    const unsigned gsrc = DY_GT + (unsigned)((h * 2 + q) * KS) * 1024u;
    int known = 0, pv = 0;
    auto fetch = [&](int s) {
      known = max(known, min_lanes8(poff == OOB ? 0x7fffffff : pv));
      if (known < s + 1) known = wait_words(rc, poff, s + 1, budget, false);
      pv = (int)ld32_sc1(rc, known < NSEG ? poff : OOB);
      v4u t[KS];
#pragma unroll
      for (int k = 0; k < KS; ++k) t[k] = ld_sc1s(rd, lane * 16u, gsrc + (unsigned)s * SEGB + k * 1024);
#pragma unroll
      for (int k = 0; k < KS; ++k) *(v4u*)(gring + (s & 1) * 14336 + k * 1024 + lane * 16) = t[k];
    };
    BARRIER();
    for (int s = 0; s < NSEG; ++s) {
      if (s > 0 && s + 1 < NSEG - 1) { __builtin_amdgcn_s_sleep(10); fetch(s + 1); }
      BARRIER();
      BARRIER();
    }
    return;
  }
  if (wave >= 4) {
    const unsigned par = read_parity(wb, lane);
    BARRIER();
    unsigned tanch = now32() - 100000u;
    for (int s = 0; s < NSEG; ++s) {
      unsigned moff = memx_off(par, s) + tid * 16; asm volatile("" : "+v"(moff));
      wait_abs(tanch + TA3);
      STAMPV(h * 4 + q, s, wave, 2);
      const int b0 = budget;
      gather_chunk(rx, moff, mimg + tid * 16, budget);
      STAMPV(h * 4 + q, s, wave, 0);
      STAMPX(s, wave, 1, 1 + b0 - budget);
      BARRIER();
      BARRIER();
      tanch = now32();
    }
    STAMPDUMP(h * 4 + q);
    return;
  }
  const int cp0 = q == 0 ? 0 : (q == 1 ? 3 : (q == 2 ? 6 : 10));
  const int i = lane & 15, g = lane >> 4, i8 = i & 7;
  const unsigned char* fbase = mimg + (g >> 1) * 256 + (i8 >> 2) * 128 + (i8 & 3) * 32 + (g & 1) * 16;
  const bool act = i < 8;
  if (HASG && wave == 3) {
    const unsigned par = read_parity(wb, lane);
    BARRIER();
    const unsigned pl = (unsigned)(512 + q * 256 + (i & 3) * 64 + g * 16);
    unsigned tanch = now32() - 100000u;
    for (int s = 0; s < NSEG - 1; ++s) {
      v4u G[KS];
      if (s > 0) __builtin_amdgcn_s_sleep(6);
#pragma unroll
      for (int k = 0; k < KS; ++k) G[k] = *(const v4u*)(gring + (s & 1) * 14336 + k * 1024 + lane * 16);
      unsigned pubo = act ? scx_off(par, s, i >> 2, h) + pl : OOB;
      unsigned moff = memx_off(par, s) + tid * 16; asm volatile("" : "+v"(moff), "+v"(pubo));
      wait_abs(tanch + TA3);
      gather_chunk(rx, moff, mimg + tid * 16, budget);
      BARRIER();
      f32x4 acc = Z4;
      v4u F[KS];
#pragma unroll
      for (int k = 0; k < 6; ++k) F[k] = *(const v4u*)(fbase + k * 512);
      __builtin_amdgcn_sched_barrier(0);
#pragma unroll
      for (int k = 0; k < KS; ++k) {
        if (k + 6 < KS) F[k + 6] = *(const v4u*)(fbase + (k + 6) * 512);
        acc = mfma32(G[k], F[k], acc);
        __builtin_amdgcn_sched_barrier(0);
      }
      st_sc1(rx, pubo, nosent(acc));
      STAMPV(h * 4 + q, s, wave, 2);
      BARRIER();
      tanch = now32();
    }
    {
      wait_abs(tanch + TA3);
      gather_chunk(rx, memx_off(par, NSEG - 1) + tid * 16, mimg + tid * 16, budget);
      BARRIER();
      BARRIER();
    }
    STAMPDUMP(h * 4 + q);
    return;
  }
  constexpr int NW = HASG ? 3 : 4;
  const int cp = cp0 + wave;
  const rsrc_t rw = mk_rsrc(wb, (unsigned)OFF_DYN), rd = mk_rsrc(wb + OFF_DYN, DYN_BYTES);
  v8u W[2][7];
#pragma unroll
  for (int t = 0; t < 2; ++t)
#pragma unroll
    for (int k = 0; k < 7; ++k) {
      const unsigned o = (unsigned)OFF_AFR + (unsigned)((h * CT + 2 * cp + t) * KS + 2 * k) * 1024u;
      W[t][k] = cat8(ld_s(rw, lane * 16u, o), ld_s(rw, lane * 16u, o + 1024u));
    }
  __builtin_amdgcn_sched_barrier(0);
  const unsigned par = read_parity(wb, lane);
  const unsigned char* abase = simg + g * 256 + (2 * (i >> 2) + (i & 1)) * 32 + ((i >> 1) & 1) * 16;
  const int sidx = (i & 2) ? 0xEEEE : 0x4444;
  unsigned char* rwr = rimg + (wave * 32 + sig(0, i)) * 16 + g * 4;
  const unsigned trbase = (unsigned)((8 * g + (i >> 2)) * 16 + (i & 1) * 8);
  const unsigned pub_l = (unsigned)(q * 128 + (i & 3) * 32 + (g & 1) * 16);
  const bool pact = act && g < 2;
  const unsigned rlane = DY_RC + (unsigned)(h * KS + cp) * 512u + (unsigned)(g * 8 + i) * 16u;
  auto trfrag = [&](int p) {
    s16x4 lo = __builtin_amdgcn_ds_read_tr16_b64_v4i16((LAS s16x4*)(rimg + trbase + p * 512));
    s16x4 hi = __builtin_amdgcn_ds_read_tr16_b64_v4i16((LAS s16x4*)(rimg + trbase + p * 512 + 64));
    v2u l2 = __builtin_bit_cast(v2u, lo), h2 = __builtin_bit_cast(v2u, hi);
    return (v4u){l2.x, l2.y, h2.x, h2.y};
  };
  if (wave == 0) STAMPW(h * 4 + q, 11);
  BARRIER();
  CP_DECL
  unsigned tanch = now32() - 100000u;
  for (int s = 0; s < NSEG; ++s) {
    unsigned pubo = (pact && s < NSEG - 1) ? scx_off(par, s, i >> 2, h) + pub_l : OOB;
    unsigned moff = memx_off(par, s) + tid * 16; asm volatile("" : "+v"(moff), "+v"(pubo));
    wait_abs(tanch + TA3);
    CP(0);
    gather_chunk(rx, moff, mimg + tid * 16, budget);
    CP(1);
    BARRIER();
    CP(2);
    f32x4 a0 = Z4, a1 = Z4;
    v4u F[7], Fc[NW];
#pragma unroll
    for (int k = 0; k < 4; ++k) F[k] = *(const v4u*)(abase + k * 1024);
    __builtin_amdgcn_sched_barrier(0);
#pragma unroll
    for (int k = 0; k < 7; ++k) {
      if (k + 4 < 7) F[k + 4] = *(const v4u*)(abase + (k + 4) * 1024);
      if (k >= 3 && k - 3 < NW) Fc[k - 3] = *(const v4u*)(fbase + (cp0 + k - 3) * 512);
      a0 = smfma64(F[k], W[0][k], a0, sidx); a1 = smfma64(F[k], W[1][k], a1, sidx);
      __builtin_amdgcn_sched_barrier(0);
    }
    STAMPV(h * 4 + q, s, wave, 0);
    *(unsigned*)(rwr) = pk2(a0[0] + a0[2], a0[1] + a0[3]);
    *(unsigned*)(rwr + 64) = pk2(a1[0] + a1[2], a1[1] + a1[3]);
    STAMPV(h * 4 + q, s, wave, 1);
    CP(3);
    BARRIER();
    CP(4);
    if (wave == 0) {
      v4u rf[NW];
#pragma unroll
      for (int p = 0; p < NW; ++p) rf[p] = trfrag(p);
      __builtin_amdgcn_sched_barrier(0);
      f32x4 acc2 = Z4;
#pragma unroll
      for (int p = 0; p < NW; ++p) acc2 = mfma32(rf[p], Fc[p], acc2);
      st_sc1(rx, pubo, nosent(acc2));
    }
    STAMPV(h * 4 + q, s, wave, 2);
    CP(5);
    tanch = now32() - (wave == 0 ? 200u : 0u);
    if (s < NSEG - 1) __builtin_amdgcn_s_sleep(8);
    const v4u rpk = trfrag(wave);
    st_sc1(rd, act ? rlane + (unsigned)s * (NH * KS * 512u) : OOB, nosentu(rpk));
  }
  CP_DUMP(h * 4 + q);
  if (budget <= 0) *(GAS unsigned*)(wb + OFF_CTRL + CT_ABORT) = 1u;
  STAMPDUMP(h * 4 + q);
}

DEV void role_O(unsigned char* wb, int rg, int cg, unsigned char* lds, int tid) {
  TUNE_LOAD
  const int lane = tid & 63, wave = rfl(tid >> 6);
  const int rho = lane & 15, g = lane >> 4;
  const rsrc_t rx = mk_rsrc(wb + OFF_XCH, (unsigned)XCH_BYTES), rd = mk_rsrc(wb + OFF_DYN, DYN_BYTES), rc = mk_rsrc(wb + OFF_CTRL, CTRL_BYTES);
  int budget = SPIN_BUDGET;
  const rsrc_t rw = mk_rsrc(wb, (unsigned)OFF_DYN);
  for (int i = 0; i < ODELAY; ++i) __builtin_amdgcn_s_sleep(16);
  v8u W[2][7]; v4u U[2], Un[2];
#pragma unroll
  for (int hh = 0; hh < 2; ++hh)
#pragma unroll
    for (int k = 0; k < 7; ++k) {
      const unsigned o = (unsigned)OFF_BTFR + (unsigned)(((2 * wave + hh) * CT + cg) * KS + 2 * k) * 1024u;
      W[hh][k] = cat8(ld_s(rw, lane * 16u, o), ld_s(rw, lane * 16u, o + 1024u));
    }
  __builtin_amdgcn_sched_barrier(0);
  const unsigned par = read_parity(wb, lane);
  const unsigned ubase = DY_UT + (unsigned)((2 * wave) * CT + cg) * 1024u;
  const unsigned poff = lane < 2 ? (unsigned)(CT_PROG + (56 + (((2 * wave + lane) * CT + cg) >> 3)) * 128) : OOB;
  int known = 0;
  if (wave == 0) STAMPW(64 + cg * 2 + rg, 11);
  U[0] = poll1(rd, ubase + lane * 16u, budget); U[1] = poll1(rd, ubase + CT * 1024 + lane * 16u, budget);
  poll1(rd, ubase + SEGB + lane * 16u, budget); poll1(rd, ubase + SEGB + CT * 1024 + lane * 16u, budget);
  known = 2;
  if (wave == 0) STAMPW(64 + cg * 2 + rg, 12);
  unsigned char* mimg = lds;
  float* red = (float*)(lds + 8192);
  const unsigned aoff = (unsigned)(g * 256 + ((rho >> 2) + 4 * (rho & 1)) * 32 + ((rho >> 1) & 1) * 16);
  const int sidx = (rho & 2) ? 0xEEEE : 0x4444;
  const int u = rho >> 3, hh_l = (rho >> 2) & 1, ii = rho & 3;
  const int hl = 2 * wave + hh_l;
  const unsigned off_mm = (unsigned)(g * 128 + ii * 32 + u * 16);
  const unsigned off_mx = (unsigned)(512 + u * 256 + ii * 64 + g * 16);
  const int wpos = (g + 4 * ii) * 20 + hl + (u ? 1280 : 0);
  const int rpos = (((lane & 15) >> 2) + 4 * (lane >> 4) + 16 * (lane & 3)) * 20;
  const unsigned publ = (unsigned)((cg * 2 + rg) * 128 + (lane >> 4) * 32 + ((lane >> 1) & 1) * 16 + ((lane >> 2) & 3) * 4);
  const unsigned stepo = (wave == 7 && lane == 0 && cg == 0 && rg == 0) ? (unsigned)CT_STEP : OOB;

  const bool vact = rg == 0 && !(g & 1);
  const unsigned vlane = DY_VC + (unsigned)((wave >> 1) * CT + cg) * 1024u + (unsigned)(((2 * wave + (g >> 1)) & 3) * 16 + rho) * 16u;
  v4u vprev = {0u, 0u, 0u, 0u};
  const int nstep = rg == 0 ? NSEG : NSEG - 1;
  CP_DECL
  unsigned tanch = now32() - 100000u;
  for (int s = 0; s < nstep; ++s) {
    {
      wait_abs(tanch + TA2);
      if (wave < 7) {
        gather_chunk(rx, memx_off(par, s) + tid * 16, mimg + tid * 16, budget);
      }
    }
    BARRIER();
    tanch = now32();
    STAMPV(64 + cg * 2 + rg, s, wave, 0);
    {
      const int sn = min(s + 1, NSEG - 2);
      if (__builtin_expect(known < sn + 1, 0)) known = wait_words(rc, poff, sn + 1, budget, false);
      Un[0] = ld_sc1s(rd, lane * 16u, ubase + (unsigned)sn * SEGB); Un[1] = ld_sc1s(rd, lane * 16u, ubase + (unsigned)sn * SEGB + CT * 1024);
      st32_sc1(rc, stepo, (unsigned)s);
      st_sc1(rd, (vact && s > 0) ? vlane + (unsigned)(s - 1) * (4u * CT * 1024u) : OOB, vprev);
    }
    __builtin_amdgcn_sched_barrier(0);
    f32x4 av0 = Z4, av1 = Z4;
    v4u af[7];
#pragma unroll
    for (int k = 0; k < 4; ++k) af[k] = *(const v4u*)(mimg + k * 1024 + aoff);
    __builtin_amdgcn_sched_barrier(0);
#pragma unroll
    for (int k = 0; k < 7; ++k) {
      if (k + 4 < 7) af[k + 4] = *(const v4u*)(mimg + (k + 4) * 1024 + aoff);
      av0 = smfma64(af[k], W[0][k], av0, sidx); av1 = smfma64(af[k], W[1][k], av1, sidx);
      __builtin_amdgcn_sched_barrier(0);
    }
    v4u vfc = {pk2(av0[0] + av0[2], av0[1] + av0[3]), pk2(av1[0] + av1[2], av1[1] + av1[3]), 0u, 0u};
    {
      auto r = __builtin_amdgcn_permlane32_swap(vfc.x, vfc.y, false, false);
      auto a = __builtin_amdgcn_permlane16_swap(r[0], r[0], false, false);
      auto b = __builtin_amdgcn_permlane16_swap(r[1], r[1], false, false);
      vprev = nosentu((v4u){a[0], a[1], b[0], b[1]});
    }
    if (s == NSEG - 1) { st_sc1(rd, vact ? vlane + (unsigned)s * (4u * CT * 1024u) : OOB, vprev); break; }
    const unsigned sb = scx_off(par, s, rg, hl);
    unsigned pubo = (lane & 1) ? OOB : memx_off(par, s + 1) + publ;
    unsigned om = sb + off_mm, ox = sb + off_mx;
    v4u bm = {0u, 0u, 0u, 0u};
    asm volatile("" : "+v"(om), "+v"(ox), "+v"(pubo), "+v"(vfc), "+v"(bm), "+v"(vprev));
    const unsigned tsc = tanch + TA1;
    wait_abs(tsc - TDX);
    v4u tx = ld_sc1(rx, ox);
    wait_abs(tsc);
    STAMPV(64 + cg * 2 + rg, s, wave, 1);
    const int b1 = budget;
    CP(0);
    v4u tm = ld_sc1(rx, om);
    __builtin_amdgcn_sched_barrier(0);
    if (__builtin_expect(any_invalid(tx), 0)) tx = poll1(rx, ox, budget);
    const float x0 = f_as(tx.x), x1 = f_as(tx.y), x2 = f_as(tx.z), x3 = f_as(tx.w);
    float mx = vmax(vmax(x0, x1), vmax(x2, x3));
    mx = vmax(mx, dppf<DPP_ROR8>(mx));
    mx = xg_max(mx);
    const float e0 = __builtin_amdgcn_exp2f(x0 - mx), e1 = __builtin_amdgcn_exp2f(x1 - mx), e2 = __builtin_amdgcn_exp2f(x2 - mx), e3 = __builtin_amdgcn_exp2f(x3 - mx);
    const unsigned q01 = pk2(e0, e1), q23 = pk2(e2, e3);
    const v4u bx = {q01, q23, dppu<DPP_ROR8>(q01), dppu<DPP_ROR8>(q23)};
    f32x4 o0 = mfma32(U[0], bx, Z4), o1 = mfma32(U[1], bx, Z4);
    float lx = (e0 + e1) + (e2 + e3);
    lx += dppf<DPP_ROR8>(lx);
    lx = xg_sum(lx);
    f32x4 ox4 = hh_l ? o1 : o0;
    asm volatile("" : "+v"(ox4), "+v"(lx), "+v"(mx));
    __builtin_amdgcn_sched_barrier(0);
    if (__builtin_expect(any_invalid(tm), 0)) tm = poll1(rx, om, budget);
    if (wave) STAMPX(s, wave, 3, 1 + b1 - budget);
    STAMPV(64 + cg * 2 + rg, s, wave, 2);
    CP(1);
    auto p0 = __builtin_amdgcn_permlane32_swap(tm.x, tm.z, false, false);
    auto p1 = __builtin_amdgcn_permlane32_swap(tm.y, tm.w, false, false);
    float a0 = f_as(p0[0]) + f_as(p0[1]), a1 = f_as(p1[0]) + f_as(p1[1]);
    auto p2 = __builtin_amdgcn_permlane16_swap(u_as(a0), u_as(a1), false, false);
    const float sm = f_as(p2[0]) + f_as(p2[1]);
    float m = vmax(vmax(sm, dppf<DPP_ROR8>(sm)), mx);
    m = xg_max(m);
    CPV(m); CP(2);
    const float em = __builtin_amdgcn_exp2f(sm - m), fx = __builtin_amdgcn_exp2f(mx - m);
    const float em2 = dppf<DPP_ROR8>(em);
    const unsigned pe = pk2(em, em2);
    bm.x = hh_l ? 0u : pe; bm.y = hh_l ? pe : 0u;
    CPV(bm); CP(3);
    ox4 = mfma32(vfc, bm, ox4 * fx);
    const float l = xg_sum(em + em2) + fx * lx;
    const float il = __builtin_amdgcn_rcpf(l);
#pragma unroll
    for (int r = 0; r < 4; ++r) red[wpos + 320 * r] = ox4[r] * il;
    CP(4);
    BARRIER();
    CP(5);
    if (wave == 0) {
      f32x4 a = *(const f32x4*)(red + rpos), b = *(const f32x4*)(red + rpos + 4), c = *(const f32x4*)(red + rpos + 8), d = *(const f32x4*)(red + rpos + 12);
      f32x4 r4 = (a + b) + (c + d);
      float v = (r4[0] + r4[1]) + (r4[2] + r4[3]);
      st32_sc1(rx, pubo, min(pk2(v, dppf<DPP_XOR1>(v)), 0xFFFFFFFEu));
    }
    tanch = now32() - (wave == 0 ? 200u : 0u);
    if (wave == 0) STAMPV(64 + cg * 2 + rg, s, wave, 3);
    CP(6);
    U[0] = Un[0]; U[1] = Un[1];
    if (known < min(NSEG - 1, s + LEAD)) {
      const int pv = poff == OOB ? 0x7fffffff : (int)ld32_sc1(rc, poff);
      known = rfl(max(known, min_lanes8(pv)));
    }
  }
  CP_DUMP(64 + cg * 2 + rg);
  if (budget <= 0) *(GAS unsigned*)(wb + OFF_CTRL + CT_ABORT) = 1u;
  STAMPDUMP(64 + cg * 2 + rg);
}

DEV float row16_max(float v) {
  v = vmax(v, dppf<DPP_XOR1>(v)); v = vmax(v, dppf<DPP_XOR2>(v));
  v = vmax(v, dppf<DPP_ROR4>(v)); v = vmax(v, dppf<DPP_ROR8>(v)); return v;
}
DEV float row16_sum(float v) {
  v += dppf<DPP_XOR1>(v); v += dppf<DPP_XOR2>(v); v += dppf<DPP_ROR4>(v); v += dppf<DPP_ROR8>(v); return v;
}
DEV int ticket(unsigned char* wb, int ctr, unsigned char* lds, int tid) {
  volatile LAS int* sl = (volatile LAS int*)(lds + 114688 - 16);
  __syncthreads();
  if (tid == 0) sl[0] = (int)__hip_atomic_fetch_add((GAS unsigned*)(wb + OFF_CTRL + ctr), 1u, __ATOMIC_RELAXED, __HIP_MEMORY_SCOPE_AGENT);
  __syncthreads();
  return rfl(sl[0]);
}
DEV void parta_unit1(const In& in, unsigned char* wb, int u, unsigned char* lds, int tid) {
  const int lane = tid & 63, wave = rfl(tid >> 6);
  const int rho = lane & 15, g = lane >> 4;
  const int h = u & 15, part = u >> 4;
  const rsrc_t rd = mk_rsrc(wb + OFF_DYN, DYN_BYTES), rw = mk_rsrc(wb, (unsigned)OFF_DYN);
#pragma unroll
  for (int i = 0; i < 7; ++i) {
    const int f = i * 8 + wave;
    const unsigned src = f < 28 ? (unsigned)OFF_WKF + h * 28672u + f * 1024u : (unsigned)OFF_WVF + h * 28672u + (f - 28) * 1024u;
    lds_dma(rw, lds + f * 1024, lane * 16u, src);
  }
  const int s = part * 8 + wave;
  const u16* xp0 = in.Ax + (size_t)(s * 32 + rho) * DM + g * 8;
  const u16* xp1 = xp0 + 16 * DM;
  v4u X0[KS], X1[KS];
#pragma unroll
  for (int k = 0; k < KS; ++k) { X0[k] = *(const v4u*)(xp0 + k * 32); X1[k] = *(const v4u*)(xp1 + k * 32); }
  DRAIN_BARRIER();
  f32x4 pk_[2][2], vv[2][2];
#pragma unroll
  for (int t = 0; t < 2; ++t)
#pragma unroll
    for (int b = 0; b < 2; ++b) { pk_[t][b] = Z4; vv[t][b] = Z4; }
#pragma unroll
  for (int k = 0; k < KS; ++k) {
    const v4u k0 = *(const v4u*)(lds + k * 1024 + lane * 16), k1 = *(const v4u*)(lds + (KS + k) * 1024 + lane * 16);
    const v4u v0 = *(const v4u*)(lds + 28672 + k * 1024 + lane * 16), v1 = *(const v4u*)(lds + 28672 + (KS + k) * 1024 + lane * 16);
    pk_[0][0] = mfma32(X0[k], k0, pk_[0][0]); pk_[0][1] = mfma32(X0[k], k1, pk_[0][1]);
    pk_[1][0] = mfma32(X1[k], k0, pk_[1][0]); pk_[1][1] = mfma32(X1[k], k1, pk_[1][1]);
    vv[0][0] = mfma32(X0[k], v0, vv[0][0]); vv[0][1] = mfma32(X0[k], v1, vv[0][1]);
    vv[1][0] = mfma32(X1[k], v0, vv[1][0]); vv[1][1] = mfma32(X1[k], v1, vv[1][1]);
  }
  const bool pad = rho >= 12;
  f32x4 pp[2][2], pn[2][2];
#pragma unroll
  for (int t = 0; t < 2; ++t)
#pragma unroll
    for (int r = 0; r < 4; ++r) {
      const float a0 = pk_[t][0][r], a1 = pad ? a0 : pk_[t][1][r];
      const float mx = row16_max(vmax(a0, a1));
      const float mn = -row16_max(vmax(-a0, -a1));
      const float ep0 = __builtin_amdgcn_exp2f(a0 - mx), ep1 = pad ? 0.f : __builtin_amdgcn_exp2f(a1 - mx);
      const float en0 = __builtin_amdgcn_exp2f(mn - a0), en1 = pad ? 0.f : __builtin_amdgcn_exp2f(mn - a1);
      const float ip = __builtin_amdgcn_rcpf(row16_sum(ep0 + ep1)), in_ = __builtin_amdgcn_rcpf(row16_sum(en0 + en1));
      pp[t][0][r] = ep0 * ip; pp[t][1][r] = ep1 * ip; pn[t][0][r] = en0 * in_; pn[t][1][r] = en1 * in_;
    }
  if (rho == 12) { vv[0][1] = (f32x4){1.f, 1.f, 1.f, 1.f}; vv[1][1] = (f32x4){1.f, 1.f, 1.f, 1.f}; }
  const v4u vb0 = pack8(vv[0][0], vv[1][0]), vb1 = pack8(vv[0][1], vv[1][1]);
  v4u fa[4] = {pack8(pp[0][0], pp[1][0]), pack8(pp[0][1], pp[1][1]), pack8(pn[0][0], pn[1][0]), pack8(pn[0][1], pn[1][1])};
  BARRIER();
  float* red = (float*)lds;
#pragma unroll
  for (int a = 0; a < 4; ++a) {
    *(f32x4*)(red + ((wave * 8 + a * 2 + 0) * 64 + lane) * 4) = mfma32(fa[a], vb0, Z4);
    *(f32x4*)(red + ((wave * 8 + a * 2 + 1) * 64 + lane) * 4) = mfma32(fa[a], vb1, Z4);
  }
  BARRIER();
  {
    f32x4 sum = Z4;
#pragma unroll
    for (int w = 0; w < 8; ++w) sum += *(const f32x4*)(red + (w * 512 + tid) * 4);
    st_sc1(rd, DY_KVP + (unsigned)(part * NH + h) * 8192u + tid * 16u, __builtin_bit_cast(v4u, sum));
  }
  DRAIN_BARRIER();
  const int arrived = ticket(wb, CT_HCNT + h * 128, lds, tid);
  if (arrived == 7) {
    if (wave < 4) {
      const int sg = wave >> 1, b = wave & 1;
      v4u lo[8], hi[8];
#pragma unroll
      for (int p = 0; p < 8; ++p) {
        lo[p] = ld_sc1s(rd, lane * 16u, DY_KVP + (unsigned)(p * NH + h) * 8192u + (unsigned)((sg * 2 + 0) * 2 + b) * 1024u);
        hi[p] = ld_sc1s(rd, lane * 16u, DY_KVP + (unsigned)(p * NH + h) * 8192u + (unsigned)((sg * 2 + 1) * 2 + b) * 1024u);
      }
      f32x4 l4 = Z4, h4 = Z4;
#pragma unroll
      for (int p = 0; p < 8; ++p) { l4 += __builtin_bit_cast(f32x4, lo[p]); h4 += __builtin_bit_cast(f32x4, hi[p]); }
      st_sc1(rd, DY_KV + (unsigned)(h * 4 + wave) * 1024u + lane * 16u, pack8(l4, h4));
    }
    DRAIN_BARRIER();
    if (tid == 0) __hip_atomic_fetch_add((GAS unsigned*)(wb + OFF_CTRL + CT_ACNT), 1u, __ATOMIC_RELAXED, __HIP_MEMORY_SCOPE_AGENT);
  }
}

DEV void parta_unit2(const In& in, unsigned char* wb, int u, unsigned char* lds, int tid) {
  const int lane = tid & 63, wave = rfl(tid >> 6);
  const int rho = lane & 15, g = lane >> 4;
  const rsrc_t rd = mk_rsrc(wb + OFF_DYN, DYN_BYTES), rc = mk_rsrc(wb + OFF_CTRL, CTRL_BYTES);
  const rsrc_t rw = mk_rsrc(wb, (unsigned)OFF_DYN);
  int budget = SPIN_BUDGET;
#pragma unroll
  for (int i = 0; i < 4; ++i) {
    const int f = i * 8 + wave;
    if (f < 28) {
      const int tt = f / KS, ks = f % KS;
      __builtin_amdgcn_global_load_lds((const unsigned*)(in.Ax + (size_t)(u * 32 + tt * 16 + rho) * DM + ks * 32 + g * 8),
                                       (LAS unsigned*)(lds + f * 1024), 16, 0, 0);
    }
  }
  unsigned char* ob_l = lds + 28672;
  f32x4 p[2][2][2];
#pragma unroll
  for (int hh = 0; hh < 2; ++hh) {
    const unsigned wq = (unsigned)OFF_WQF + (unsigned)(2 * wave + hh) * 28672u;
    v4u Wf[2][KS];
    asm volatile("" ::: "memory");
#pragma unroll
    for (int k = 0; k < KS; ++k) { Wf[0][k] = ld_s(rw, lane * 16u, wq + k * 1024); Wf[1][k] = ld_s(rw, lane * 16u, wq + (KS + k) * 1024); }
    if (hh == 0) DRAIN_BARRIER();
    p[hh][0][0] = Z4; p[hh][0][1] = Z4; p[hh][1][0] = Z4; p[hh][1][1] = Z4;
#pragma unroll
    for (int k = 0; k < KS; ++k) {
      const v4u x0 = *(const v4u*)(lds + k * 1024 + lane * 16), x1 = *(const v4u*)(lds + (KS + k) * 1024 + lane * 16);
      p[hh][0][0] = mfma32(Wf[0][k], x0, p[hh][0][0]); p[hh][1][0] = mfma32(Wf[1][k], x0, p[hh][1][0]);
      p[hh][0][1] = mfma32(Wf[0][k], x1, p[hh][0][1]); p[hh][1][1] = mfma32(Wf[1][k], x1, p[hh][1][1]);
    }
    PIN2(p[hh][0][0], p[hh][0][1]); PIN2(p[hh][1][0], p[hh][1][1]);
  }
  wait_words(rc, lane == 0 ? (unsigned)CT_ACNT : OOB, NH, budget, true);
  const bool pad = g == 3;
#pragma unroll
  for (int hh = 0; hh < 2; ++hh) {
    const int h = 2 * wave + hh;
    v4u kvf[4];
#pragma unroll
    for (int a = 0; a < 4; ++a) kvf[a] = ld_sc1s(rd, lane * 16u, DY_KV + (unsigned)(h * 4 + a) * 1024u);
#pragma unroll
    for (int tt = 0; tt < 2; ++tt) {
      const f32x4 p0 = p[hh][0][tt], p1 = pad ? p0 : p[hh][1][tt];
      float mx = vmax(vmax(vmax(p0[0], p0[1]), vmax(p0[2], p0[3])), vmax(vmax(p1[0], p1[1]), vmax(p1[2], p1[3])));
      float mn = vmax(vmax(vmax(-p0[0], -p0[1]), vmax(-p0[2], -p0[3])), vmax(vmax(-p1[0], -p1[1]), vmax(-p1[2], -p1[3])));
      mx = xg_max(mx); mn = -xg_max(mn);
      f32x4 ep0, ep1, en0, en1;
      float sp = 0.f, sn = 0.f;
#pragma unroll
      for (int r = 0; r < 4; ++r) {
        ep0[r] = __builtin_amdgcn_exp2f(p0[r] - mx); ep1[r] = pad ? 0.f : __builtin_amdgcn_exp2f(p1[r] - mx);
        en0[r] = __builtin_amdgcn_exp2f(mn - p0[r]); en1[r] = pad ? 0.f : __builtin_amdgcn_exp2f(mn - p1[r]);
        sp += ep0[r] + ep1[r]; sn += en0[r] + en1[r];
      }
      const float ip = __builtin_amdgcn_rcpf(xg_sum(sp)), in_ = __builtin_amdgcn_rcpf(xg_sum(sn));
      const v4u bp = pack8(ep0 * ip, ep1 * ip), bn = pack8(en0 * in_, en1 * in_);
      f32x4 n0 = mfma32(kvf[0], bp, Z4), n1 = mfma32(kvf[1], bp, Z4);
      n0 = mfma32(kvf[2], bn, n0); n1 = mfma32(kvf[3], bn, n1);
      const float den = xg_sum(pad ? n1[0] : 0.f) + 1e-6f;
      const float id = 1.f / den;
      if (pad) n1 = Z4;
      *(v4u*)(ob_l + (h * 2 + tt) * 1024 + lane * 16) = pack8(n0 * id, n1 * id);
    }
  }
  const int wv = wave < 7 ? wave : 6;
  const unsigned wo = (unsigned)OFF_WOF + (unsigned)(wv * 4) * 1024u;
  f32x4 acc[4][2];
#pragma unroll
  for (int n = 0; n < 4; ++n) { acc[n][0] = Z4; acc[n][1] = Z4; }
#pragma unroll
  for (int hb = 0; hb < 2; ++hb) {
    v4u Wo_[8][4];
    asm volatile("" ::: "memory");
#pragma unroll
    for (int h8 = 0; h8 < 8; ++h8)
#pragma unroll
      for (int n = 0; n < 4; ++n) Wo_[h8][n] = ld_s(rw, lane * 16u, wo + (unsigned)((hb * 8 + h8) * CT + n) * 1024u);
    if (hb == 0) BARRIER();
#pragma unroll
    for (int h8 = 0; h8 < 8; ++h8) {
      const int h = hb * 8 + h8;
      const v4u o0 = *(const v4u*)(ob_l + (h * 2) * 1024 + lane * 16), o1 = *(const v4u*)(ob_l + (h * 2 + 1) * 1024 + lane * 16);
#pragma unroll
      for (int n = 0; n < 4; ++n) { acc[n][0] = mfma32(Wo_[h8][n], o0, acc[n][0]); acc[n][1] = mfma32(Wo_[h8][n], o1, acc[n][1]); }
    }
#pragma unroll
    for (int n = 0; n < 4; ++n) PIN2(acc[n][0], acc[n][1]);
  }
  if (wave < 7) {
#pragma unroll
    for (int tt = 0; tt < 2; ++tt)
#pragma unroll
      for (int np = 0; np < 2; ++np)
        *(v4u*)(in.out + (size_t)(u * 32 + tt * 16 + rho) * 896 + (wave * 2 + np) * 32 + 8 * g) = pack8(acc[2 * np][tt], acc[2 * np + 1][tt]);
  }
}

DEV void parta_all(const In& in, unsigned char* wb, unsigned char* lds, int tid) {
  for (;;) { const int u = ticket(wb, CT_Q1, lds, tid); if (u >= 128) break; parta_unit1(in, wb, u, lds, tid); }
  for (;;) { const int u = ticket(wb, CT_Q2, lds, tid); if (u >= 64) break; parta_unit2(in, wb, u, lds, tid); }
}

DEV void unit_XS(unsigned char* wb, int s, int hq, unsigned char* lds, int tid) {
  const int lane = tid & 63, wave = rfl(tid >> 6);
  const int rho = lane & 15, g = lane >> 4;
  const int h = hq * 4 + (wave >> 1), tt = wave & 1;
  const rsrc_t rw = mk_rsrc(wb, (unsigned)OFF_DYN);
  const rsrc_t rd = mk_rsrc(wb + OFF_DYN, DYN_BYTES), rc = mk_rsrc(wb + OFF_CTRL, CTRL_BYTES);
  int budget = SPIN_BUDGET;
  const int tok = sig(tt, rho);
  v4u X[KS];
#pragma unroll
  for (int k = 0; k < KS; ++k) X[k] = ld_s(rw, lane * 16u, (unsigned)OFF_XFR2 + (unsigned)((s * 2 + tt) * KS + k) * 1024u);
  if (wave == 0) wait_words(rc, lane < 14 ? (unsigned)(CT_PROG + (hq * 14 + lane) * 128) : OOB, s + 1, budget, true);
  BARRIER();
  const unsigned gp = DY_GT + (unsigned)s * SEGB + (unsigned)(h * 2 * KS) * 1024u;
  f32x4 sx0 = Z4, sx1 = Z4, sm = Z4;
  {
    v4u G0[KS], G1[KS];
#pragma unroll
    for (int k = 0; k < KS; ++k) { G0[k] = ld_sc1s(rd, lane * 16u, gp + k * 1024); G1[k] = ld_sc1s(rd, lane * 16u, gp + (KS + k) * 1024); }
#pragma unroll
    for (int k = 0; k < KS; ++k) { sx0 = mfma32(G0[k], X[k], sx0); sx1 = mfma32(G1[k], X[k], sx1); }
    PIN2(sx0, sx1);
  }
  if (wave == 0) {
    wait_words(rc, lane == 0 ? (unsigned)CT_STEP : OOB, s < NSEG - 2 ? s + 2 : NSEG - 2, budget, true);
  }
  BARRIER();
  {
    const int key = ((rho & 7) >> 1) + 4 * (rho & 1);
    const unsigned rl = (unsigned)(g * 8 + key) * 16u;
    if (s >= NSEG - 2) poll1(rd, lane < KS ? DY_RC + (unsigned)((s * NH + h) * KS + lane) * 512u : OOB, budget);
    v4u R[KS];
    bool bad;
#pragma nounroll
    do {
      asm volatile("" ::: "memory");
      unsigned mm = 0;
#pragma unroll
      for (int k = 0; k < KS; ++k) R[k] = ld_sc1s(rd, rl, DY_RC + (unsigned)((s * NH + h) * KS + k) * 512u);
#pragma unroll
      for (int k = 0; k < KS; ++k) mm = max(mm, max(max(R[k].x, R[k].y), max(R[k].z, R[k].w)));
      bad = __any(mm == 0xFFFFFFFFu);
      budget -= 4;
    } while (bad && budget > 0);
#pragma unroll
    for (int k = 0; k < KS; ++k) sm = mfma32(R[k], X[k], sm);
    if (s == 63) STAMPG(hq, 14);
  }
  float v[8], w[4];
  float m = -3.0e38f;
#pragma unroll
  for (int j = 0; j < 8; ++j) { v[j] = (8 * g + j <= tok) ? (j < 4 ? sx0[j & 3] : sx1[j & 3]) : -3.0e38f; m = vmax(m, v[j]); }
#pragma unroll
  for (int j = 0; j < 4; ++j) { w[j] = g < 2 ? sm[j] : -3.0e38f; m = vmax(m, w[j]); }
  m = xg_max(m);
  float l = 0.f;
#pragma unroll
  for (int j = 0; j < 8; ++j) { v[j] = (8 * g + j <= tok) ? __builtin_amdgcn_exp2f(v[j] - m) : 0.f; l += v[j]; }
#pragma unroll
  for (int j = 0; j < 4; ++j) { w[j] = g < 2 ? __builtin_amdgcn_exp2f(w[j] - m) : 0.f; l += w[j]; }
  l = xg_sum(l);
  const float il = __builtin_amdgcn_rcpf(l);
  const v4u px = {pk2(v[0] * il, v[1] * il), pk2(v[2] * il, v[3] * il), pk2(v[4] * il, v[5] * il), pk2(v[6] * il, v[7] * il)};
  const v2u pm = {min(pk2(w[0] * il, w[1] * il), 0xFFFFFFFEu), min(pk2(w[2] * il, w[3] * il), 0xFFFFFFFEu)};
  st_sc1(rd, DY_PX + (unsigned)((s * NH + h) * 2 + tt) * 1024u + lane * 16u, nosentu(px));
  __builtin_amdgcn_raw_buffer_store_b64(pm, rd, g < 2 ? DY_PM + (unsigned)((s * 4 + hq) * 2 + tt) * 1024u + (unsigned)((h & 3) * 16 + rho) * 16u + 8u * g : OOB, 0, 16);
  if (s == 63) STAMPG(hq, 15);
  DRAIN_BARRIER();
  if (tid == 0) __hip_atomic_fetch_add((GAS unsigned*)(wb + OFF_CTRL + CT_XCNT + s * 128), 1u, __ATOMIC_RELAXED, __HIP_MEMORY_SCOPE_AGENT);
}

DEV void unit_XO(const In& in, unsigned char* wb, int s, int qt, unsigned char* lds, int tid) {
  const int lane = tid & 63, wave = rfl(tid >> 6);
  const int rho = lane & 15, g = lane >> 4;
  const rsrc_t rd = mk_rsrc(wb + OFF_DYN, DYN_BYTES), rc = mk_rsrc(wb + OFF_CTRL, CTRL_BYTES);
  int budget = SPIN_BUDGET;
  const int ct = qt * 7 + (wave < 7 ? wave : 6);
  if (wave == 0) wait_words(rc, lane < 56 ? (unsigned)(CT_PROG + (56 + lane) * 128) : OOB, s + 1, budget, true);
  BARRIER();
  v4u U[NH];
#pragma unroll
  for (int h = 0; h < NH; ++h) U[h] = ld_sc1s(rd, lane * 16u, DY_UT + (unsigned)s * SEGB + (unsigned)(h * CT + ct) * 1024u);
  if (wave == 0) {
    wait_words(rc, lane == 0 ? (unsigned)CT_STEP : OOB, s < NSEG - 2 ? s + 2 : NSEG - 2, budget, true);
  }
  BARRIER();
  v4u V[4];
  {
    bool bad;
#pragma nounroll
    do {
      asm volatile("" ::: "memory");
      unsigned mm = 0;
#pragma unroll
      for (int hq = 0; hq < 4; ++hq) V[hq] = ld_sc1s(rd, lane * 16u, DY_VC + (unsigned)((s * 4 + hq) * CT + ct) * 1024u);
#pragma unroll
      for (int hq = 0; hq < 4; ++hq) mm = max(mm, max(max(V[hq].x, V[hq].y), max(V[hq].z, V[hq].w)));
      bad = __any(mm == 0xFFFFFFFFu);
      budget -= 2;
    } while (bad && budget > 0);
  }
  if (s == 63) STAMPG(qt, 16);
  if (wave == 0 && s < NSEG - 2) wait_words(rc, lane == 0 ? (unsigned)(CT_XCNT + s * 128) : OOB, 4, budget, true);
  BARRIER();
  {
    if (s >= NSEG - 2) poll1(rd, lane < 32 ? DY_PX + (unsigned)s * 32768u + lane * 1024u : OOB, budget);
    v4u t[5];
    bool bad;
#pragma nounroll
    do {
      asm volatile("" ::: "memory");
      unsigned mm = 0;
#pragma unroll
      for (int i = 0; i < 4; ++i) t[i] = ld_sc1s(rd, tid * 16u, DY_PX + (unsigned)s * 32768u + i * 8192u);
      t[4] = ld_sc1s(rd, tid * 16u, DY_PM + (unsigned)s * 8192u);
#pragma unroll
      for (int i = 0; i < 5; ++i) mm = max(mm, max(max(t[i].x, t[i].y), max(t[i].z, t[i].w)));
      bad = __any(mm == 0xFFFFFFFFu);
      budget -= 2;
    } while (bad && budget > 0);
#pragma unroll
    for (int i = 0; i < 5; ++i) *(v4u*)(lds + (i * 512 + tid) * 16) = t[i];
  }
  BARRIER();
  if (s == 63) STAMPG(qt, 17);
  f32x4 a0 = Z4, a1 = Z4;
#pragma unroll
  for (int h = 0; h < NH; ++h) {
    a0 = mfma32(U[h], *(const v4u*)(lds + (h * 2) * 1024 + lane * 16), a0);
    a1 = mfma32(U[h], *(const v4u*)(lds + (h * 2 + 1) * 1024 + lane * 16), a1);
  }
#pragma unroll
  for (int hq = 0; hq < 4; ++hq) {
    a0 = mfma32(V[hq], *(const v4u*)(lds + 32768 + (hq * 2) * 1024 + lane * 16), a0);
    a1 = mfma32(V[hq], *(const v4u*)(lds + 32768 + (hq * 2 + 1) * 1024 + lane * 16), a1);
  }
  if (wave < 7) {
    v2u o0 = {pk2(a0[0], a0[1]), pk2(a0[2], a0[3])}, o1 = {pk2(a1[0], a1[1]), pk2(a1[2], a1[3])};
    *(v2u*)(in.out + (size_t)(s * 32 + sig(0, rho)) * 896 + 448 + ct * 16 + 4 * g) = o0;
    *(v2u*)(in.out + (size_t)(s * 32 + sig(1, rho)) * 896 + 448 + ct * 16 + 4 * g) = o1;
  }
}

DEV int get_slot(unsigned char* wb, unsigned char* lds, int tid) {
  int* sl = (int*)lds;
  if (tid == 0) {
    GAS unsigned* tick = (GAS unsigned*)(wb + OFF_CTRL + CT_TICK);
    const unsigned x = xcc_id() & 7u;
    const unsigned r = __hip_atomic_fetch_add(tick + x * 64, 1u, __ATOMIC_RELAXED, __HIP_MEMORY_SCOPE_AGENT);
    int slot;
    if (r < 32) slot = (int)(x * 32 + r);
    else {
      unsigned c[8]; int spins = 0;
      for (;;) {
        unsigned tot = 0;
#pragma unroll
        for (int k = 0; k < 8; ++k) { c[k] = __hip_atomic_load(tick + k * 64, __ATOMIC_RELAXED, __HIP_MEMORY_SCOPE_AGENT); tot += c[k]; }
        if (tot >= 256u || ++spins > SPIN_BUDGET) break;
      }
      unsigned idx = r - 32;
#pragma unroll
      for (int k = 0; k < 8; ++k) idx += ((unsigned)k < x && c[k] > 32) ? c[k] - 32 : 0;
      slot = -1;
#pragma unroll
      for (int k = 0; k < 8; ++k) {
        const unsigned fr = c[k] < 32 ? 32 - c[k] : 0;
        if (slot < 0) { if (idx < fr) slot = (int)(k * 32 + c[k] + idx); else idx -= fr; }
      }
    }
    sl[0] = slot;
  }
  __syncthreads();
  const int slot = rfl(sl[0]);
  __syncthreads();
  return slot;
}

constexpr int MAIN_LDS = 114688;
__global__ void __launch_bounds__(512, 2) k_main(In in) {
  extern __shared__ __attribute__((aligned(16))) unsigned char lds[];
  unsigned char* wb = ws_base(in.ws);
  const int tid = threadIdx.x;
  const int slot = get_slot(wb, lds, tid);
  if (slot < 0) return;
  STAMPG(slot, 0);
  const int idx = (slot & 31) * 4 + ((slot >> 5) & 3);
  if (slot < 128) {
    if (idx < 64) {
      const int h = idx >> 2, q = idx & 3;
      if (q < 2) role_S<true>(wb, h, q, lds, tid); else role_S<false>(wb, h, q, lds, tid);
    } else if (idx < 120) {
      const int o = idx - 64;
      role_O(wb, (o >> 2) & 1, (o >> 3) * 4 + (o & 3), lds, tid);
    }
  } else {
    if (idx < 56) gu_worker<true>(wb, idx, lds, tid);
    else if (idx < 112) gu_worker<false>(wb, idx, lds, tid);
    else for (int i = 0; i < MDELAY; ++i) __builtin_amdgcn_s_sleep(16);
    parta_all(in, wb, lds, tid);
    STAMPG(slot, 2);
    for (;;) {
      const int u = ticket(wb, CT_Q3, lds, tid);
      if (u >= NSEG * 8) break;
      if (u & 4) unit_XO(in, wb, u >> 3, u & 3, lds, tid); else unit_XS(wb, u >> 3, u & 3, lds, tid);
      if (u >= NSEG * 8 - 16) break;
    }
  }
  STAMPG(slot, 3);
}

extern "C" void kernel_launch(void* const* d_in, const int* in_sizes, int n_in,
                              void* d_out, int out_size, void* d_ws, size_t ws_size,
                              hipStream_t stream) {
  In in;
  in.Ax = (const u16*)d_in[0]; in.Awqkv = (const u16*)d_in[1]; in.Awphi = (const u16*)d_in[2]; in.Awo = (const u16*)d_in[3];
  in.Bx = (const u16*)d_in[4]; in.Bmem0 = (const u16*)d_in[5]; in.Wq = (const u16*)d_in[6]; in.Wk = (const u16*)d_in[7];
  in.Wv = (const u16*)d_in[8]; in.Wo = (const u16*)d_in[9];
  in.out = (u16*)d_out; in.ws = d_ws;
  (void)hipFuncSetAttribute((const void*)k_prep, hipFuncAttributeMaxDynamicSharedMemorySize, 57344);
  (void)hipFuncSetAttribute((const void*)k_main, hipFuncAttributeMaxDynamicSharedMemorySize, MAIN_LDS);
  k_prep<<<PREP_GRID, 448, 57344, stream>>>(in);
  k_main<<<256, 512, MAIN_LDS, stream>>>(in);
}
```

```cpp
#include <hip/hip_runtime.h>
#include <hip/hip_bf16.h>

typedef unsigned short u16;
typedef __bf16 bf16x8 __attribute__((ext_vector_type(8)));
typedef __bf16 bf16x2 __attribute__((ext_vector_type(2)));
typedef short s16x4 __attribute__((ext_vector_type(4)));
typedef float f32x4 __attribute__((ext_vector_type(4)));
typedef unsigned v4u __attribute__((ext_vector_type(4)));
typedef unsigned v2u __attribute__((ext_vector_type(2)));
typedef unsigned v8u __attribute__((ext_vector_type(8)));
typedef __bf16 bf16x16 __attribute__((ext_vector_type(16)));
#define DEV __device__ __forceinline__
#define LAS __attribute__((address_space(3)))
#define GAS __attribute__((address_space(1)))

constexpr int DM = 448, NH = 16, EH = 256, NSEG = 64, KS = 14, CT = 28;
constexpr size_t MB = 1u << 20;
constexpr size_t OFF_XCH = 0;
constexpr size_t XCH_BYTES = 6 * MB;
constexpr int XCH_PAIRS_USED = 316;
constexpr size_t OFF_CTRL = 6 * MB;
constexpr size_t OFF_AFR = 8 * MB;
constexpr size_t OFF_BTFR = 16 * MB;
constexpr size_t OFF_XFR = 24 * MB;
constexpr size_t OFF_WQF = 26 * MB;
constexpr size_t OFF_WKF = 27 * MB;
constexpr size_t OFF_WVF = 28 * MB;
constexpr size_t OFF_WOF = 29 * MB;
constexpr size_t OFF_DYN = 32 * MB;
constexpr unsigned DY_GT = 0;
constexpr unsigned DY_UT = 32u << 20;
constexpr unsigned DY_RC = 64u << 20;
constexpr unsigned DY_VC = 72u << 20;
constexpr unsigned DY_PX = 80u << 20;
constexpr unsigned DY_PM = 84u << 20;
constexpr unsigned DY_KV = 86u << 20;
constexpr unsigned DY_KVP = 87u << 20;
constexpr unsigned DYN_BYTES = 88u << 20;
constexpr unsigned SEGB = NH * CT * 1024;
constexpr unsigned OOB = 0xFFFFFF00u;
constexpr int SPIN_BUDGET = 1 << 20;
constexpr int CT_VOTE = 0;
constexpr int CT_TICK = 4096;
constexpr int CT_ABORT = 8192;
constexpr int CT_STEP = 8448;
constexpr int CT_ACNT = 8704;
constexpr int CT_Q1 = 8960;
constexpr int CT_Q2 = 9216;
constexpr int CT_HCNT = 10240;
constexpr int CT_PROG = 16384;
constexpr int CT_Q3 = 9472;
constexpr int CT_FILL = 12544;
constexpr int CT_XCNT = 32768;
constexpr int CTRL_BYTES = 40960;

DEV unsigned char* ws_base(void* ws) { const size_t a = (size_t)ws; return (unsigned char*)ws + (((a + (2 * MB - 1)) & ~(size_t)(2 * MB - 1)) - a); }
DEV unsigned pk2(float a, float b) { bf16x2 v = {(__bf16)a, (__bf16)b}; return __builtin_bit_cast(unsigned, v); }
DEV f32x4 mfma32(v4u a, v4u b, f32x4 c) { return __builtin_amdgcn_mfma_f32_16x16x32_bf16(__builtin_bit_cast(bf16x8, a), __builtin_bit_cast(bf16x8, b), c, 0, 0, 0); }
DEV f32x4 smfma64(v4u a, v8u b, f32x4 c, int idx) { return __builtin_amdgcn_smfmac_f32_16x16x64_bf16(__builtin_bit_cast(bf16x8, a), __builtin_bit_cast(bf16x16, b), c, idx, 0, 0); }
DEV v8u cat8(v4u a, v4u b) { return __builtin_shufflevector(a, b, 0, 1, 2, 3, 4, 5, 6, 7); }
DEV v4u pack8(f32x4 a, f32x4 b) { v4u r; r.x = pk2(a[0], a[1]); r.y = pk2(a[2], a[3]); r.z = pk2(b[0], b[1]); r.w = pk2(b[2], b[3]); return r; }
DEV int sig(int tau, int rho) { return 8 * (rho >> 2) + 4 * tau + (rho & 3); }
DEV int sig2(int tau, int rho) { const int g = rho >> 2, r = rho & 3; return 16 * (g >> 1) + 8 * tau + 4 * (r >> 1) + 2 * (g & 1) + (r & 1); }
DEV unsigned xcc_id() { return (unsigned)__builtin_amdgcn_s_getreg(20 | (3 << 11)) & 0xfu; }
DEV int rfl(int x) { return __builtin_amdgcn_readfirstlane(x); }
DEV float f_as(unsigned x) { return __builtin_bit_cast(float, x); }
DEV unsigned u_as(float x) { return __builtin_bit_cast(unsigned, x); }
template <int CTRL> DEV float dppf(float x) { return f_as((unsigned)__builtin_amdgcn_update_dpp(0, (int)u_as(x), CTRL, 0xf, 0xf, true)); }
template <int CTRL> DEV unsigned dppu(unsigned x) { return (unsigned)__builtin_amdgcn_update_dpp(0, (int)x, CTRL, 0xf, 0xf, true); }
constexpr int DPP_ROR8 = 0x128, DPP_ROR4 = 0x124, DPP_XOR1 = 0xB1, DPP_XOR2 = 0x4E;
DEV float vmax(float a, float b) { return __builtin_elementwise_maximum(a, b); }
DEV float xg_max(float m) {
  auto a = __builtin_amdgcn_permlane32_swap(u_as(m), u_as(m), false, false);
  m = vmax(f_as(a[0]), f_as(a[1]));
  auto b = __builtin_amdgcn_permlane16_swap(u_as(m), u_as(m), false, false);
  return vmax(f_as(b[0]), f_as(b[1]));
}
DEV float xg_sum(float m) {
  auto a = __builtin_amdgcn_permlane32_swap(u_as(m), u_as(m), false, false);
  m = f_as(a[0]) + f_as(a[1]);
  auto b = __builtin_amdgcn_permlane16_swap(u_as(m), u_as(m), false, false);
  return f_as(b[0]) + f_as(b[1]);
}
typedef __amdgpu_buffer_rsrc_t rsrc_t;
DEV rsrc_t mk_rsrc(void* p, unsigned bytes) { return __builtin_amdgcn_make_buffer_rsrc(p, 0, (int)bytes, 0x00020000); }
DEV v4u ld_sc1(rsrc_t r, unsigned off) { return __builtin_amdgcn_raw_buffer_load_b128(r, off, 0, 16); }
DEV v4u ld_sc1s(rsrc_t r, unsigned voff, unsigned soff) { return __builtin_amdgcn_raw_buffer_load_b128(r, voff, soff, 16); }
DEV v4u ld_s(rsrc_t r, unsigned voff, unsigned soff) { return __builtin_amdgcn_raw_buffer_load_b128(r, voff, soff, 0); }
DEV void lds_dma(rsrc_t r, unsigned char* ldsp, unsigned voff, unsigned soff) { __builtin_amdgcn_raw_ptr_buffer_load_lds(r, (LAS void*)ldsp, 16, voff, soff, 0, 0); }
DEV void st_sc1(rsrc_t r, unsigned off, v4u v) { __builtin_amdgcn_raw_buffer_store_b128(v, r, off, 0, 16); }
DEV unsigned ld32_sc1(rsrc_t r, unsigned off) { return __builtin_amdgcn_raw_buffer_load_b32(r, off, 0, 16); }
DEV void st32_sc1(rsrc_t r, unsigned off, unsigned v) { __builtin_amdgcn_raw_buffer_store_b32(v, r, off, 0, 16); }
DEV unsigned long long memtime() { unsigned long long t; asm volatile("s_memtime %0\n\ts_waitcnt lgkmcnt(0)" : "=s"(t) :: "memory"); return t; }
#define GUDELAY 24
#define LEAD 5
#define ODELAY 0
#define MDELAY 12
constexpr unsigned TA1 = 1230u;
constexpr unsigned TA2 = 650u;
constexpr unsigned TA3 = 2800u;
constexpr unsigned TDX = 400u;
DEV unsigned now32() { return (unsigned)__builtin_amdgcn_s_memtime(); }
DEV void wait_abs(unsigned t) { while ((int)(now32() - t) < 0) {} }
DEV v4u nosentu(v4u r) { v4u o = {min(r.x, 0xFFFFFFFEu), min(r.y, 0xFFFFFFFEu), min(r.z, 0xFFFFFFFEu), min(r.w, 0xFFFFFFFEu)}; return o; }
DEV v4u nosent(f32x4 r) { v4u o = {min(u_as(r[0]), 0xFFFFFFFEu), min(u_as(r[1]), 0xFFFFFFFEu), min(u_as(r[2]), 0xFFFFFFFEu), min(u_as(r[3]), 0xFFFFFFFEu)}; return o; }
#define BARRIER() asm volatile("s_waitcnt lgkmcnt(0)\n\ts_barrier" ::: "memory")
#define DRAIN_BARRIER() asm volatile("s_waitcnt vmcnt(0) lgkmcnt(0)\n\ts_barrier" ::: "memory")
#define Z4 ((f32x4){0.f, 0.f, 0.f, 0.f})
#define PIN2(a, b) asm volatile("" : "+v"(a), "+v"(b) :: "memory")

struct In {
  const u16 *Ax, *Awqkv, *Awphi, *Awo, *Bx, *Bmem0, *Wq, *Wk, *Wv, *Wo;
  u16* out; void* ws;
};

DEV v4u ld16_a8(const u16* p) { v2u a = *(const v2u*)p, b = *(const v2u*)(p + 4); return (v4u){a.x, a.y, b.x, b.y}; }

DEV void prep_parta_unit(const In& in, unsigned char* wb, int u, int lane) {
  const int rho = lane & 15, g = lane >> 4;
  if (u < 3 * NH * KS) {
    const int ks = u % KS, h = (u / KS) % NH, which = u / (KS * NH);
    v4u a[2], b[2];
#pragma unroll
    for (int t = 0; t < 2; ++t) {
      const int k = 32 * ks + sig(t, rho);
      const u16* src = in.Awqkv + (size_t)k * 1344 + which * 448 + h * 28 + 8 * g;
      v2u lo = *(const v2u*)src, hi = {0u, 0u};
      if (g < 3) hi = *(const v2u*)(src + 4);
      a[t] = (v4u){lo.x, lo.y, hi.x, hi.y};
    }
#pragma unroll
    for (int t = 0; t < 2; ++t) {
      const int f = sig(t, rho);
      unsigned e[8];
#pragma unroll
      for (int j = 0; j < 8; ++j) {
        const int d = 8 * g + j;
        unsigned v = 0;
        if (d < 28 && f < 28) v = which == 2 ? (d == f ? 0x3F80u : 0u) : (unsigned)in.Awphi[(h * 28 + d) * 28 + f];
        e[j] = v;
      }
      b[t] = (v4u){e[0] | (e[1] << 16), e[2] | (e[3] << 16), e[4] | (e[5] << 16), e[6] | (e[7] << 16)};
    }
    const float sc = which == 2 ? 1.f : 1.4426950408889634f;
    unsigned char* dst = wb + (which == 0 ? OFF_WQF : (which == 1 ? OFF_WKF : OFF_WVF));
#pragma unroll
    for (int t = 0; t < 2; ++t) {
      f32x4 d0 = mfma32(a[0], b[t], Z4), d1 = mfma32(a[1], b[t], Z4);
      *(v4u*)(dst + ((size_t)(h * 2 + t) * KS + ks) * 1024 + lane * 16) = pack8(d0 * sc, d1 * sc);
    }
  } else {
    const int v = u - 3 * NH * KS, np = v % KS, h = v / KS;
    v4u a[2];
#pragma unroll
    for (int t = 0; t < 2; ++t) {
      const int d = sig(t, rho);
      a[t] = (v4u){0u, 0u, 0u, 0u};
      if (d < 28) a[t] = *(const v4u*)(in.Awo + (size_t)(h * 28 + d) * DM + np * 32 + 8 * g);
    }
#pragma unroll
    for (int t = 0; t < 2; ++t) {
      const int n = sig(t, rho) - 8 * g;
      unsigned e[4] = {0u, 0u, 0u, 0u};
#pragma unroll
      for (int j = 0; j < 4; ++j) e[j] = (n == 2 * j ? 0x3F80u : 0u) | (n == 2 * j + 1 ? 0x3F800000u : 0u);
      const v4u b = {e[0], e[1], e[2], e[3]};
      f32x4 d0 = mfma32(a[0], b, Z4), d1 = mfma32(a[1], b, Z4);
      *(v4u*)(wb + OFF_WOF + (size_t)(h * CT + np * 2 + t) * 1024 + lane * 16) = pack8(d0, d1);
    }
  }
}

#define PAUX 16
#define PST(r, off, v) __builtin_amdgcn_raw_buffer_store_b128(v, r, off, 0, PAUX)
constexpr unsigned FSTEPS = 8;
constexpr unsigned NFILL = 48;
constexpr int PREP_GRID = 256 + 128 + 37 + NFILL;
__global__ void __launch_bounds__(448, 4) k_prep(In in) {
  extern __shared__ __attribute__((aligned(16))) unsigned char lds[];
  unsigned char* wb = ws_base(in.ws);
  const int tid = threadIdx.x, lane = tid & 63, wave = rfl(tid >> 6), b = blockIdx.x;
  const int rho = lane & 15, g = lane >> 4;
  if (b >= 421) {
    const rsrc_t rw = mk_rsrc(wb, (unsigned)OFF_DYN), rd = mk_rsrc(wb + OFF_DYN, DYN_BYTES);
    const unsigned nt16 = NFILL * 448u * 16u, t0 = (unsigned)((b - 421) * 448 + tid);
    for (unsigned o = t0; o < (CTRL_BYTES - CT_TICK) / 4; o += NFILL * 448u) *(unsigned*)(wb + OFF_CTRL + CT_TICK + o * 4) = 0u;
    const v4u ones = {~0u, ~0u, ~0u, ~0u};
    for (unsigned o2 = t0 * 16u; o2 < (FSTEPS + 1 + 4 * FSTEPS) * 16384u; o2 += nt16) {
      const unsigned o = o2 < (FSTEPS + 1) * 16384u ? o2 : o2 + (64u - (FSTEPS + 1)) * 16384u;
      v4u v = ones;
      if (o < 16384) {
        const int w = (int)(o & 8191);
        if (w < 7168) {
          const int slot = w >> 7, cg = slot >> 1, rg = slot & 1, ii = (w >> 5) & 3, hf = (w >> 4) & 1;
          const unsigned* mp = (const unsigned*)(in.Bmem0 + (rg * 4 + ii) * DM + cg * 16) + hf;
          v = nosentu((v4u){mp[0], mp[2], mp[4], mp[6]});
        }
      }
      PST(rw, (unsigned)OFF_XCH + o, v);
    }
    const unsigned RB = NH * KS * 512u;
    for (unsigned o = t0 * 16u; o < 2u * SEGB; o += nt16) { PST(rd, DY_GT + o, ones); PST(rd, DY_UT + o, ones); }
    for (unsigned o = t0 * 16u; o < 2u * RB; o += nt16) { PST(rd, DY_RC + 62u * RB + o, ones); PST(rd, DY_VC + 62u * RB + o, ones); }
    for (unsigned o = t0 * 16u; o < 65536u; o += nt16) PST(rd, DY_PX + 62u * 32768u + o, ones);
    for (unsigned o = t0 * 16u; o < 16384u; o += nt16) PST(rd, DY_PM + 62u * 8192u + o, ones);
    return;
  }
  if (b >= 384) {
    const int pi = (b - 384) * 7 + wave;
    if (lane == 0 && pi < 256) {
      const int pair = (pi * 3) >> 1;
      unsigned* p0 = (unsigned*)(wb + OFF_XCH + (size_t)pair * 16384 + 8064);
      unsigned* p1 = p0 + 2048;
      unsigned t0 = ~0u, t1 = ~0u;
      for (int rep = 0; rep < 3; ++rep) {
        unsigned v, v2, z = 0;
        unsigned long long a = memtime();
        asm volatile("global_atomic_or %0, %1, %2, off sc0\n\ts_waitcnt vmcnt(0)" : "=v"(v) : "v"(p0), "v"(z) : "memory");
        unsigned long long bb = memtime();
        asm volatile("global_atomic_or %0, %1, %2, off sc0\n\ts_waitcnt vmcnt(0)" : "=v"(v2) : "v"(p1), "v"(z) : "memory");
        unsigned long long c = memtime();
        t0 = min(t0, (unsigned)(bb - a)); t1 = min(t1, (unsigned)(c - bb));
      }
      const int vote = (t1 < t0) ^ (xcc_id() >= 4);
      const unsigned d = t0 > t1 ? t0 - t1 : t1 - t0;
      ((int*)(wb + OFF_CTRL + CT_VOTE))[pi] = (pair >> 7) * 4 + (d > 100 ? vote : 2);
    }
    return;
  }
  if (b >= 256) { prep_parta_unit(in, wb, (b - 256) * 7 + wave, lane); return; }
  const int mat = (b & 7) * 4 + (b >> 6), sub = (b >> 3) & 7;
  const int m = mat >> 4, h = mat & 15, rh = sub >> 2, cq = sub & 3;
  const rsrc_t rw = mk_rsrc(wb, (unsigned)OFF_DYN);
  const rsrc_t rq = mk_rsrc((void*)(m == 0 ? in.Wq : in.Wo), 448u * 4096u * 2u);
  const rsrc_t rp_ = mk_rsrc((void*)(m == 0 ? in.Wk : in.Wv), 448u * 4096u * 2u);
  const int rp = rh * 7 + wave;
  unsigned qv[8];
  if (m == 0) {
    const int T = cq * 7 + wave, k = 32 * (T >> 1) + sig2(T & 1, rho);
#pragma unroll
    for (int es = 0; es < 8; ++es) qv[es] = (unsigned)(k * 4096 + h * EH + es * 32 + g * 8) * 2u;
  } else {
#pragma unroll
    for (int i = 0; i < 8; ++i) {
      const int n = ((i >> 2) * 28 + wave * 4 + (i & 3)) * 64 + lane, e = n / 14, ch = n % 14;
      qv[i] = (unsigned)((h * EH + e) * DM + cq * 112 + ch * 8) * 2u;
    }
  }
  const unsigned pv0 = (unsigned)((32 * rp + sig(0, rho)) * 4096 + h * EH + g * 8) * 2u, pv1 = pv0 + 4u * 4096u * 2u;
  v4u pa[2][8];
#pragma unroll
  for (int hf = 0; hf < 2; ++hf) {
#pragma unroll
    for (int i = 0; i < 4; ++i) {
      const int slot = m == 0 ? wave * 8 + hf * 4 + i : hf * 28 + wave * 4 + i;
      lds_dma(rq, lds + slot * 1024, qv[hf * 4 + i], 0u);
    }
#pragma unroll
    for (int i = 0; i < 4; ++i) { pa[0][hf * 4 + i] = ld_s(rp_, pv0, (hf * 4 + i) * 64u); pa[1][hf * 4 + i] = ld_s(rp_, pv1, (hf * 4 + i) * 64u); }
  }
  v4u xv;
  {
    const int j = b * 448 + tid, l = j & 63, f = j >> 6, cs = f % KS, tau = (f / KS) & 1, s = f / (2 * KS);
    xv = *(const v4u*)(in.Bx + (size_t)(s * 32 + sig(tau, l & 15)) * DM + cs * 32 + (l >> 4) * 8);
  }
  {
    const float sc = m ? 1.f : 0.0625f * 1.4426950408889634f;
    const unsigned dst = (unsigned)(m ? OFF_BTFR : OFF_AFR) + (unsigned)((h * CT + cq * 7) * KS + rp) * 1024u + lane * 16u;
    const unsigned troff = (unsigned)((8 * g + (rho >> 2)) * 224 + (rho & 3) * 8);
    DRAIN_BARRIER();
    if (m == 0) {
      v4u qb[2][8];
#pragma unroll
      for (int es = 0; es < 8; ++es) qb[0][es] = *(const v4u*)(lds + es * 1024 + lane * 16);
#pragma unroll
      for (int i = 0; i < 7; ++i) {
        f32x4 a0 = Z4, a1 = Z4;
#pragma unroll
        for (int es = 0; es < 8; ++es) {
          if (i + 1 < 7 && (es & 1) == 0) {
            qb[(i + 1) & 1][es] = *(const v4u*)(lds + ((i + 1) * 8 + es) * 1024 + lane * 16);
          }
          a0 = mfma32(pa[0][es], qb[i & 1][es], a0); a1 = mfma32(pa[1][es], qb[i & 1][es], a1);
          if (i + 1 < 7 && (es & 1) == 1) {
            qb[(i + 1) & 1][es] = *(const v4u*)(lds + ((i + 1) * 8 + es) * 1024 + lane * 16);
          }
        }
        PST(rw, dst + i * (KS * 1024u), pack8(a0 * sc, a1 * sc));
      }
    } else {
#pragma unroll
      for (int i = 0; i < 7; ++i) {
        f32x4 a0 = Z4, a1 = Z4;
#pragma unroll
        for (int es = 0; es < 8; ++es) {
          s16x4 lo = __builtin_amdgcn_ds_read_tr16_b64_v4i16((LAS s16x4*)(lds + troff + es * 32 * 224 + i * 32));
          s16x4 hi = __builtin_amdgcn_ds_read_tr16_b64_v4i16((LAS s16x4*)(lds + troff + (es * 32 + 4) * 224 + i * 32));
          v2u l2 = __builtin_bit_cast(v2u, lo), h2 = __builtin_bit_cast(v2u, hi);
          const v4u qb = {l2.x, l2.y, h2.x, h2.y};
          a0 = mfma32(pa[0][es], qb, a0); a1 = mfma32(pa[1][es], qb, a1);
        }
        PST(rw, dst + i * (KS * 1024u), pack8(a0, a1));
      }
    }
  }
  PST(rw, (unsigned)OFF_XFR + (unsigned)(b * 448 + tid) * 16u, xv);
}

DEV unsigned read_parity(const unsigned char* wb, int lane) {
  const int* v = (const int*)(wb + OFF_CTRL + CT_VOTE);
  int x[4];
#pragma unroll
  for (int k = 0; k < 4; ++k) x[k] = v[k * 64 + lane];
  unsigned par = 0;
#pragma unroll
  for (int p = 0; p < 3; ++p) {
    int n1 = 0, n0 = 0;
#pragma unroll
    for (int k = 0; k < 4; ++k) { n1 += __popcll(__ballot(x[k] == p * 4 + 1)); n0 += __popcll(__ballot(x[k] == p * 4)); }
    par |= (n1 > n0 ? 1u : 0u) << p;
  }
  return par;
}
DEV unsigned pair_off(unsigned par, int p) { return (unsigned)p * 16384u + (((par >> (p >> 7)) & 1u) << 13); }
DEV unsigned memx_off(unsigned par, int s) { return pair_off(par, s); }
DEV unsigned scx_off(unsigned par, int s, int rg, int h) { return pair_off(par, 64 + 4 * s + 2 * rg + (h >> 3)) + (h & 7) * 1024; }
DEV bool any_invalid(v4u v) { unsigned m = max(max(v.x, v.y), max(v.z, v.w)); return __any(m == 0xFFFFFFFFu); }
DEV v4u poll1(rsrc_t rx, unsigned off, int& budget) {
  v4u t;
#pragma nounroll
  do { asm volatile("" ::: "memory"); t = ld_sc1(rx, off); --budget; } while (any_invalid(t) && budget > 0);
  return t;
}
DEV void gather_chunk(rsrc_t rx, unsigned off, unsigned char* dst, int& budget) {
  v4u t = ld_sc1(rx, off);
  *(v4u*)dst = t;
  if (__builtin_expect(any_invalid(t), 0)) { t = poll1(rx, off, budget); *(v4u*)dst = t; }
}
DEV int min_lanes8(int v) {
  v = min(v, (int)dppu<DPP_XOR1>((unsigned)v)); v = min(v, (int)dppu<DPP_XOR2>((unsigned)v));
  return min(__builtin_amdgcn_readlane(v, 0), __builtin_amdgcn_readlane(v, 4));
}
DEV int wait_words(rsrc_t rc, unsigned off, int need, int& budget, bool sleep) {
  int mn;
  for (;;) {
    asm volatile("" ::: "memory");
    const int v = off == OOB ? 0x7fffffff : (int)ld32_sc1(rc, off);
    --budget;
    if (__all(v >= need) || budget <= 0) {
      mn = need;
      if (__all(v >= need + 16)) mn = need + 16; else if (__all(v >= need + 4)) mn = need + 4;
      break;
    }
    if (sleep) { __builtin_amdgcn_s_sleep(8); budget -= 4; }
  }
  return mn;
}

#define FILL_GUARD(s) do { if (__builtin_expect((s) == (int)FSTEPS - 2, 0)) wait_words(mk_rsrc(wb + OFF_CTRL, CTRL_BYTES), lane == 0 ? (unsigned)CT_FILL : OOB, 8, budget, false); } while (0)

template <bool ISG>
DEV void gu_compute(const v4u (&W)[2][KS], const unsigned char* xb, v4u& r0, v4u& r1) {
  f32x4 a00 = Z4, a01 = Z4, a10 = Z4, a11 = Z4;
  v4u x0[KS], x1[KS];
#pragma unroll
  for (int k = 0; k < 2; ++k) { x0[k] = *(const v4u*)(xb + k * 1024); x1[k] = *(const v4u*)(xb + (KS + k) * 1024); }
  __builtin_amdgcn_sched_barrier(0);
#pragma unroll
  for (int k = 0; k < KS; ++k) {
    if (k + 2 < KS) { x0[k + 2] = *(const v4u*)(xb + (k + 2) * 1024); x1[k + 2] = *(const v4u*)(xb + (KS + k + 2) * 1024); }
    if (ISG) {
      a00 = mfma32(W[0][k], x0[k], a00); a10 = mfma32(W[1][k], x0[k], a10);
      a01 = mfma32(W[0][k], x1[k], a01); a11 = mfma32(W[1][k], x1[k], a11);
    } else {
      a00 = mfma32(x0[k], W[0][k], a00); a10 = mfma32(x0[k], W[1][k], a10);
      a01 = mfma32(x1[k], W[0][k], a01); a11 = mfma32(x1[k], W[1][k], a11);
    }
    __builtin_amdgcn_sched_barrier(0);
  }
  if (ISG) { r0 = pack8(a00, a10); r1 = pack8(a01, a11); } else { r0 = pack8(a00, a01); r1 = pack8(a10, a11); }
}

template <bool ISG>
DEV void gu_worker(unsigned char* wb, int w, unsigned char* lds, int tid) {
  const int lane = tid & 63, wave = rfl(tid >> 6), tp = wave & 3, sp = wave >> 2;
  const unsigned wfr = (unsigned)(ISG ? OFF_AFR : OFF_BTFR);
  const rsrc_t rw = mk_rsrc(wb, (unsigned)OFF_DYN);
  const rsrc_t rd = mk_rsrc(wb + OFF_DYN, DYN_BYTES), rc = mk_rsrc(wb + OFF_CTRL, CTRL_BYTES);
  const int T = 8 * (ISG ? w : w - 56) + 2 * tp;
  const int h = T / CT, tl = T % CT;
  auto stage = [&](int s, int buf) {
#pragma unroll
    for (int i = 0; i < 7; ++i)
      lds_dma(rw, lds + buf * 57344 + i * 8192 + wave * 1024, tid * 16u, (unsigned)OFF_XFR + (unsigned)s * 28672u + i * 8192u);
  };
  stage(0, 0);
  __builtin_amdgcn_sched_barrier(0);
  const unsigned o0 = (ISG ? (unsigned)((h * 2 + 0) * KS + (tl >> 1)) : (unsigned)T) * 1024u + lane * 16u;
  const unsigned o1 = (ISG ? (unsigned)((h * 2 + 1) * KS + (tl >> 1)) : (unsigned)(T + 1)) * 1024u + lane * 16u;
  if (sp == 1) {
    asm volatile("s_waitcnt vmcnt(0)\n\ts_barrier" ::: "memory");
    __builtin_amdgcn_s_sleep(GUDELAY);
  }
  v4u W[2][KS];
#pragma unroll
  for (int k = 0; k < KS; ++k)
#pragma unroll
    for (int t = 0; t < 2; ++t) W[t][k] = ld_s(rw, lane * 16u, wfr + (unsigned)((T + t) * KS + k) * 1024u);
  __builtin_amdgcn_sched_barrier(0);
  if (sp == 0) {
    asm volatile("s_waitcnt vmcnt(28)\n\ts_barrier" ::: "memory");
#pragma nounroll
    for (int sg = 0; sg < 2; ++sg) {
      v4u r0, r1;
      gu_compute<ISG>(W, lds + sg * 28672 + lane * 16, r0, r1);
      const unsigned ob = (ISG ? DY_GT : DY_UT) + (unsigned)sg * SEGB;
      st_sc1(rd, ob + o0, nosentu(r0)); st_sc1(rd, ob + o1, nosentu(r1));
    }
  }
  stage(2, 1);
  int buf = 1;
  for (int s = 2; s < NSEG; s += 2) {
    if (s <= 4) asm volatile("s_waitcnt vmcnt(0) lgkmcnt(0)\n\ts_barrier" ::: "memory");
    else asm volatile("s_waitcnt vmcnt(2) lgkmcnt(0)\n\ts_barrier" ::: "memory");
    if (wave == 7) st32_sc1(rc, lane == 0 ? (unsigned)(CT_PROG + w * 128) : OOB, (unsigned)(s <= 4 ? s : s - 2));
    stage(s + 2 < NSEG ? s + 2 : s, buf ^ 1);
    v4u r0, r1;
    gu_compute<ISG>(W, lds + buf * 57344 + sp * 28672 + lane * 16, r0, r1);
    const unsigned ob = (ISG ? DY_GT : DY_UT) + (unsigned)(s + sp) * SEGB;
    st_sc1(rd, ob + o0, nosentu(r0)); st_sc1(rd, ob + o1, nosentu(r1));
    buf ^= 1;
  }
  DRAIN_BARRIER();
  if (wave == 7) st32_sc1(rc, lane == 0 ? (unsigned)(CT_PROG + w * 128) : OOB, (unsigned)NSEG);
}

template <bool HASG>
DEV void role_S(unsigned char* wb, int h, int q, unsigned char* lds, int tid) {
  const int lane = tid & 63, wave = rfl(tid >> 6);
  unsigned char* rimg = lds;
  unsigned char* mimg = lds + 4096;
  unsigned char* simg = mimg;
  unsigned char* gring = lds + 20480;
  const rsrc_t rx = mk_rsrc(wb + OFF_XCH, (unsigned)XCH_BYTES);
  int budget = SPIN_BUDGET;
  if (HASG && wave >= 4) {
    const rsrc_t rd = mk_rsrc(wb + OFF_DYN, DYN_BYTES);
    const int f0 = (wave - 4) * 7;
    const unsigned src = DY_GT + (unsigned)((h * 2 + q) * KS + (f0 % KS)) * 1024u + (unsigned)(f0 / KS) * SEGB;
    v4u t[7];
    bool bad;
#pragma nounroll
    do {
      asm volatile("" ::: "memory");
      unsigned m = 0;
#pragma unroll
      for (int k = 0; k < 7; ++k) t[k] = ld_sc1s(rd, lane * 16u, src + k * 1024u);
#pragma unroll
      for (int k = 0; k < 7; ++k) m = max(m, max(max(t[k].x, t[k].y), max(t[k].z, t[k].w)));
      bad = __any(m == 0xFFFFFFFFu);
      budget -= 2;
    } while (bad && budget > 0);
#pragma unroll
    for (int k = 0; k < 7; ++k) *(v4u*)(gring + (f0 + k) * 1024 + lane * 16) = t[k];
  }
  if (wave == 7) {
    if (!HASG) { BARRIER(); return; }
    const rsrc_t rd = mk_rsrc(wb + OFF_DYN, DYN_BYTES), rc = mk_rsrc(wb + OFF_CTRL, CTRL_BYTES);
    const int w0 = (28 * h) >> 3, w1 = (28 * h + 27) >> 3;
    const unsigned poff = (w0 + lane <= w1) ? (unsigned)(CT_PROG + (w0 + lane) * 128) : OOB;
    const unsigned gsrc = DY_GT + (unsigned)((h * 2 + q) * KS) * 1024u;
    int known = 0, pv = 0;
    auto fetch = [&](int s) {
      known = max(known, min_lanes8(poff == OOB ? 0x7fffffff : pv));
      if (known < s + 1) known = wait_words(rc, poff, s + 1, budget, false);
      pv = (int)ld32_sc1(rc, known < NSEG ? poff : OOB);
      v4u t[KS];
#pragma unroll
      for (int k = 0; k < KS; ++k) t[k] = ld_sc1s(rd, lane * 16u, gsrc + (unsigned)s * SEGB + k * 1024);
#pragma unroll
      for (int k = 0; k < KS; ++k) *(v4u*)(gring + (s & 1) * 14336 + k * 1024 + lane * 16) = t[k];
    };
    BARRIER();
    for (int s = 0; s < NSEG; ++s) {
      if (s > 0 && s + 1 < NSEG - 1) { __builtin_amdgcn_s_sleep(10); fetch(s + 1); }
      BARRIER();
      BARRIER();
    }
    return;
  }
  if (wave >= 4) {
    const unsigned par = read_parity(wb, lane);
    BARRIER();
    unsigned tanch = now32() - 100000u;
    for (int s = 0; s < NSEG; ++s) {
      unsigned moff = memx_off(par, s) + tid * 16; asm volatile("" : "+v"(moff));
      wait_abs(tanch + TA3);
      gather_chunk(rx, moff, mimg + tid * 16, budget);
      BARRIER();
      BARRIER();
      tanch = now32();
      FILL_GUARD(s);
    }
    return;
  }
  const int cp0 = q == 0 ? 0 : (q == 1 ? 3 : (q == 2 ? 6 : 10));
  const int i = lane & 15, g = lane >> 4, i8 = i & 7;
  const unsigned char* fbase = mimg + (g >> 1) * 256 + (i8 >> 2) * 128 + (i8 & 3) * 32 + (g & 1) * 16;
  const bool act = i < 8;
  if (HASG && wave == 3) {
    const unsigned par = read_parity(wb, lane);
    BARRIER();
    const unsigned pl = (unsigned)(512 + q * 256 + (i & 3) * 64 + g * 16);
    unsigned tanch = now32() - 100000u;
    for (int s = 0; s < NSEG - 1; ++s) {
      v4u G[KS];
      if (s > 0) __builtin_amdgcn_s_sleep(6);
#pragma unroll
      for (int k = 0; k < KS; ++k) G[k] = *(const v4u*)(gring + (s & 1) * 14336 + k * 1024 + lane * 16);
      unsigned pubo = act ? scx_off(par, s, i >> 2, h) + pl : OOB;
      unsigned moff = memx_off(par, s) + tid * 16; asm volatile("" : "+v"(moff), "+v"(pubo));
      wait_abs(tanch + TA3);
      gather_chunk(rx, moff, mimg + tid * 16, budget);
      BARRIER();
      f32x4 acc = Z4;
      v4u F[KS];
#pragma unroll
      for (int k = 0; k < 6; ++k) F[k] = *(const v4u*)(fbase + k * 512);
      __builtin_amdgcn_sched_barrier(0);
#pragma unroll
      for (int k = 0; k < KS; ++k) {
        if (k + 6 < KS) F[k + 6] = *(const v4u*)(fbase + (k + 6) * 512);
        acc = mfma32(G[k], F[k], acc);
        __builtin_amdgcn_sched_barrier(0);
      }
      st_sc1(rx, pubo, nosent(acc));
      BARRIER();
      tanch = now32();
      FILL_GUARD(s);
    }
    {
      wait_abs(tanch + TA3);
      gather_chunk(rx, memx_off(par, NSEG - 1) + tid * 16, mimg + tid * 16, budget);
      BARRIER();
      BARRIER();
    }
    return;
  }
  constexpr int NW = HASG ? 3 : 4;
  const int cp = cp0 + wave;
  const rsrc_t rw = mk_rsrc(wb, (unsigned)OFF_DYN), rd = mk_rsrc(wb + OFF_DYN, DYN_BYTES);
  v8u W[2][7];
#pragma unroll
  for (int t = 0; t < 2; ++t)
#pragma unroll
    for (int k = 0; k < 7; ++k) {
      const unsigned o = (unsigned)OFF_AFR + (unsigned)((h * CT + 2 * cp + t) * KS + 2 * k) * 1024u;
      W[t][k] = cat8(ld_s(rw, lane * 16u, o), ld_s(rw, lane * 16u, o + 1024u));
    }
  __builtin_amdgcn_sched_barrier(0);
  const unsigned par = read_parity(wb, lane);
  const unsigned char* abase = simg + g * 256 + (2 * (i >> 2) + (i & 1)) * 32 + ((i >> 1) & 1) * 16;
  const int sidx = (i & 2) ? 0xEEEE : 0x4444;
  unsigned char* rwr = rimg + (wave * 32 + sig(0, i)) * 16 + g * 4;
  const unsigned trbase = (unsigned)((8 * g + (i >> 2)) * 16 + (i & 1) * 8);
  const unsigned pub_l = (unsigned)(q * 128 + (i & 3) * 32 + (g & 1) * 16);
  const bool pact = act && g < 2;
  const unsigned rlane = DY_RC + (unsigned)(h * KS + cp) * 512u + (unsigned)(g * 8 + i) * 16u;
  auto trfrag = [&](int p) {
    s16x4 lo = __builtin_amdgcn_ds_read_tr16_b64_v4i16((LAS s16x4*)(rimg + trbase + p * 512));
    s16x4 hi = __builtin_amdgcn_ds_read_tr16_b64_v4i16((LAS s16x4*)(rimg + trbase + p * 512 + 64));
    v2u l2 = __builtin_bit_cast(v2u, lo), h2 = __builtin_bit_cast(v2u, hi);
    return (v4u){l2.x, l2.y, h2.x, h2.y};
  };
  BARRIER();
  unsigned tanch = now32() - 100000u;
  for (int s = 0; s < NSEG; ++s) {
    unsigned pubo = (pact && s < NSEG - 1) ? scx_off(par, s, i >> 2, h) + pub_l : OOB;
    unsigned moff = memx_off(par, s) + tid * 16; asm volatile("" : "+v"(moff), "+v"(pubo));
    wait_abs(tanch + TA3);
    gather_chunk(rx, moff, mimg + tid * 16, budget);
    BARRIER();
    f32x4 a0 = Z4, a1 = Z4;
    v4u F[7], Fc[NW];
#pragma unroll
    for (int k = 0; k < 4; ++k) F[k] = *(const v4u*)(abase + k * 1024);
    __builtin_amdgcn_sched_barrier(0);
#pragma unroll
    for (int k = 0; k < 7; ++k) {
      if (k + 4 < 7) F[k + 4] = *(const v4u*)(abase + (k + 4) * 1024);
      if (k >= 3 && k - 3 < NW) Fc[k - 3] = *(const v4u*)(fbase + (cp0 + k - 3) * 512);
      a0 = smfma64(F[k], W[0][k], a0, sidx); a1 = smfma64(F[k], W[1][k], a1, sidx);
      __builtin_amdgcn_sched_barrier(0);
    }
    *(unsigned*)(rwr) = pk2(a0[0] + a0[2], a0[1] + a0[3]);
    *(unsigned*)(rwr + 64) = pk2(a1[0] + a1[2], a1[1] + a1[3]);
    BARRIER();
    if (wave == 0) {
      v4u rf[NW];
#pragma unroll
      for (int p = 0; p < NW; ++p) rf[p] = trfrag(p);
      __builtin_amdgcn_sched_barrier(0);
      f32x4 acc2 = Z4;
#pragma unroll
      for (int p = 0; p < NW; ++p) acc2 = mfma32(rf[p], Fc[p], acc2);
      st_sc1(rx, pubo, nosent(acc2));
    }
    tanch = now32() - (wave == 0 ? 200u : 0u);
    if (s < NSEG - 1) __builtin_amdgcn_s_sleep(8);
    const v4u rpk = trfrag(wave);
    st_sc1(rd, act ? rlane + (unsigned)s * (NH * KS * 512u) : OOB, nosentu(rpk));
    FILL_GUARD(s);
  }
  if (budget <= 0) *(GAS unsigned*)(wb + OFF_CTRL + CT_ABORT) = 1u;
}

DEV void role_O(unsigned char* wb, int rg, int cg, unsigned char* lds, int tid) {
  const int lane = tid & 63, wave = rfl(tid >> 6);
  const int rho = lane & 15, g = lane >> 4;
  const rsrc_t rx = mk_rsrc(wb + OFF_XCH, (unsigned)XCH_BYTES), rd = mk_rsrc(wb + OFF_DYN, DYN_BYTES), rc = mk_rsrc(wb + OFF_CTRL, CTRL_BYTES);
  int budget = SPIN_BUDGET;
  const rsrc_t rw = mk_rsrc(wb, (unsigned)OFF_DYN);
  for (int i = 0; i < ODELAY; ++i) __builtin_amdgcn_s_sleep(16);
  v8u W[2][7]; v4u U[2], Un[2];
#pragma unroll
  for (int hh = 0; hh < 2; ++hh)
#pragma unroll
    for (int k = 0; k < 7; ++k) {
      const unsigned o = (unsigned)OFF_BTFR + (unsigned)(((2 * wave + hh) * CT + cg) * KS + 2 * k) * 1024u;
      W[hh][k] = cat8(ld_s(rw, lane * 16u, o), ld_s(rw, lane * 16u, o + 1024u));
    }
  __builtin_amdgcn_sched_barrier(0);
  const unsigned par = read_parity(wb, lane);
  const unsigned ubase = DY_UT + (unsigned)((2 * wave) * CT + cg) * 1024u;
  const unsigned poff = lane < 2 ? (unsigned)(CT_PROG + (56 + (((2 * wave + lane) * CT + cg) >> 3)) * 128) : OOB;
  int known = 0;
  U[0] = poll1(rd, ubase + lane * 16u, budget); U[1] = poll1(rd, ubase + CT * 1024 + lane * 16u, budget);
  poll1(rd, ubase + SEGB + lane * 16u, budget); poll1(rd, ubase + SEGB + CT * 1024 + lane * 16u, budget);
  known = 2;
  unsigned char* mimg = lds;
  float* red = (float*)(lds + 8192);
  const unsigned aoff = (unsigned)(g * 256 + ((rho >> 2) + 4 * (rho & 1)) * 32 + ((rho >> 1) & 1) * 16);
  const int sidx = (rho & 2) ? 0xEEEE : 0x4444;
  const int u = rho >> 3, hh_l = (rho >> 2) & 1, ii = rho & 3;
  const int hl = 2 * wave + hh_l;
  const unsigned off_mm = (unsigned)(g * 128 + ii * 32 + u * 16);
  const unsigned off_mx = (unsigned)(512 + u * 256 + ii * 64 + g * 16);
  const int wpos = (g + 4 * ii) * 20 + hl + (u ? 1280 : 0);
  const int rpos = (((lane & 15) >> 2) + 4 * (lane >> 4) + 16 * (lane & 3)) * 20;
  const unsigned publ = (unsigned)((cg * 2 + rg) * 128 + (lane >> 4) * 32 + ((lane >> 1) & 1) * 16 + ((lane >> 2) & 3) * 4);
  const unsigned stepo = (wave == 7 && lane == 0 && cg == 0 && rg == 0) ? (unsigned)CT_STEP : OOB;

  const bool vact = rg == 0 && !(g & 1);
  const unsigned vlane = DY_VC + (unsigned)((wave >> 1) * CT + cg) * 1024u + (unsigned)(((2 * wave + (g >> 1)) & 3) * 16 + rho) * 16u;
  v4u vprev = {0u, 0u, 0u, 0u};
  const int nstep = rg == 0 ? NSEG : NSEG - 1;
  unsigned tanch = now32() - 100000u;
  for (int s = 0; s < nstep; ++s) {
    {
      wait_abs(tanch + TA2);
      if (wave < 7) {
        gather_chunk(rx, memx_off(par, s) + tid * 16, mimg + tid * 16, budget);
      }
    }
    BARRIER();
    tanch = now32();
    {
      const int sn = min(s + 1, NSEG - 2);
      if (__builtin_expect(known < sn + 1, 0)) known = wait_words(rc, poff, sn + 1, budget, false);
      Un[0] = ld_sc1s(rd, lane * 16u, ubase + (unsigned)sn * SEGB); Un[1] = ld_sc1s(rd, lane * 16u, ubase + (unsigned)sn * SEGB + CT * 1024);
      st32_sc1(rc, stepo, (unsigned)s);
      st_sc1(rd, (vact && s > 0) ? vlane + (unsigned)(s - 1) * (4u * CT * 1024u) : OOB, vprev);
    }
    __builtin_amdgcn_sched_barrier(0);
    f32x4 av0 = Z4, av1 = Z4;
    v4u af[7];
#pragma unroll
    for (int k = 0; k < 4; ++k) af[k] = *(const v4u*)(mimg + k * 1024 + aoff);
    __builtin_amdgcn_sched_barrier(0);
#pragma unroll
    for (int k = 0; k < 7; ++k) {
      if (k + 4 < 7) af[k + 4] = *(const v4u*)(mimg + (k + 4) * 1024 + aoff);
      av0 = smfma64(af[k], W[0][k], av0, sidx); av1 = smfma64(af[k], W[1][k], av1, sidx);
      __builtin_amdgcn_sched_barrier(0);
    }
    v4u vfc = {pk2(av0[0] + av0[2], av0[1] + av0[3]), pk2(av1[0] + av1[2], av1[1] + av1[3]), 0u, 0u};
    {
      auto r = __builtin_amdgcn_permlane32_swap(vfc.x, vfc.y, false, false);
      auto a = __builtin_amdgcn_permlane16_swap(r[0], r[0], false, false);
      auto b = __builtin_amdgcn_permlane16_swap(r[1], r[1], false, false);
      vprev = nosentu((v4u){a[0], a[1], b[0], b[1]});
    }
    if (s == NSEG - 1) { st_sc1(rd, vact ? vlane + (unsigned)s * (4u * CT * 1024u) : OOB, vprev); break; }
    const unsigned sb = scx_off(par, s, rg, hl);
    unsigned pubo = (lane & 1) ? OOB : memx_off(par, s + 1) + publ;
    unsigned om = sb + off_mm, ox = sb + off_mx;
    v4u bm = {0u, 0u, 0u, 0u};
    asm volatile("" : "+v"(om), "+v"(ox), "+v"(pubo), "+v"(vfc), "+v"(bm), "+v"(vprev));
    const unsigned tsc = tanch + TA1;
    wait_abs(tsc - TDX);
    v4u tx = ld_sc1(rx, ox);
    wait_abs(tsc);
    v4u tm = ld_sc1(rx, om);
    __builtin_amdgcn_sched_barrier(0);
    if (__builtin_expect(any_invalid(tx), 0)) tx = poll1(rx, ox, budget);
    const float x0 = f_as(tx.x), x1 = f_as(tx.y), x2 = f_as(tx.z), x3 = f_as(tx.w);
    float mx = vmax(vmax(x0, x1), vmax(x2, x3));
    mx = vmax(mx, dppf<DPP_ROR8>(mx));
    mx = xg_max(mx);
    const float e0 = __builtin_amdgcn_exp2f(x0 - mx), e1 = __builtin_amdgcn_exp2f(x1 - mx), e2 = __builtin_amdgcn_exp2f(x2 - mx), e3 = __builtin_amdgcn_exp2f(x3 - mx);
    const unsigned q01 = pk2(e0, e1), q23 = pk2(e2, e3);
    const v4u bx = {q01, q23, dppu<DPP_ROR8>(q01), dppu<DPP_ROR8>(q23)};
    f32x4 o0 = mfma32(U[0], bx, Z4), o1 = mfma32(U[1], bx, Z4);
    float lx = (e0 + e1) + (e2 + e3);
    lx += dppf<DPP_ROR8>(lx);
    lx = xg_sum(lx);
    f32x4 ox4 = hh_l ? o1 : o0;
    asm volatile("" : "+v"(ox4), "+v"(lx), "+v"(mx));
    __builtin_amdgcn_sched_barrier(0);
    if (__builtin_expect(any_invalid(tm), 0)) tm = poll1(rx, om, budget);
    auto p0 = __builtin_amdgcn_permlane32_swap(tm.x, tm.z, false, false);
    auto p1 = __builtin_amdgcn_permlane32_swap(tm.y, tm.w, false, false);
    float a0 = f_as(p0[0]) + f_as(p0[1]), a1 = f_as(p1[0]) + f_as(p1[1]);
    auto p2 = __builtin_amdgcn_permlane16_swap(u_as(a0), u_as(a1), false, false);
    const float sm = f_as(p2[0]) + f_as(p2[1]);
    float m = vmax(vmax(sm, dppf<DPP_ROR8>(sm)), mx);
    m = xg_max(m);
    const float em = __builtin_amdgcn_exp2f(sm - m), fx = __builtin_amdgcn_exp2f(mx - m);
    const float em2 = dppf<DPP_ROR8>(em);
    const unsigned pe = pk2(em, em2);
    bm.x = hh_l ? 0u : pe; bm.y = hh_l ? pe : 0u;
    ox4 = mfma32(vfc, bm, ox4 * fx);
    const float l = xg_sum(em + em2) + fx * lx;
    const float il = __builtin_amdgcn_rcpf(l);
#pragma unroll
    for (int r = 0; r < 4; ++r) red[wpos + 320 * r] = ox4[r] * il;
    BARRIER();
    if (wave == 0) {
      f32x4 a = *(const f32x4*)(red + rpos), b = *(const f32x4*)(red + rpos + 4), c = *(const f32x4*)(red + rpos + 8), d = *(const f32x4*)(red + rpos + 12);
      f32x4 r4 = (a + b) + (c + d);
      float v = (r4[0] + r4[1]) + (r4[2] + r4[3]);
      st32_sc1(rx, pubo, min(pk2(v, dppf<DPP_XOR1>(v)), 0xFFFFFFFEu));
    }
    tanch = now32() - (wave == 0 ? 200u : 0u);
    U[0] = Un[0]; U[1] = Un[1];
    FILL_GUARD(s);
    if (known < min(NSEG - 1, s + LEAD)) {
      const int pv = poff == OOB ? 0x7fffffff : (int)ld32_sc1(rc, poff);
      known = rfl(max(known, min_lanes8(pv)));
    }
  }
  if (budget <= 0) *(GAS unsigned*)(wb + OFF_CTRL + CT_ABORT) = 1u;
}

DEV float row16_max(float v) {
  v = vmax(v, dppf<DPP_XOR1>(v)); v = vmax(v, dppf<DPP_XOR2>(v));
  v = vmax(v, dppf<DPP_ROR4>(v)); v = vmax(v, dppf<DPP_ROR8>(v)); return v;
}
DEV float row16_sum(float v) {
  v += dppf<DPP_XOR1>(v); v += dppf<DPP_XOR2>(v); v += dppf<DPP_ROR4>(v); v += dppf<DPP_ROR8>(v); return v;
}
DEV int ticket(unsigned char* wb, int ctr, unsigned char* lds, int tid) {
  volatile LAS int* sl = (volatile LAS int*)(lds + 114688 - 16);
  __syncthreads();
  if (tid == 0) sl[0] = (int)__hip_atomic_fetch_add((GAS unsigned*)(wb + OFF_CTRL + ctr), 1u, __ATOMIC_RELAXED, __HIP_MEMORY_SCOPE_AGENT);
  __syncthreads();
  return rfl(sl[0]);
}
DEV void parta_unit1(const In& in, unsigned char* wb, int u, unsigned char* lds, int tid) {
  const int lane = tid & 63, wave = rfl(tid >> 6);
  const int rho = lane & 15, g = lane >> 4;
  const int h = u & 15, part = u >> 4;
  const rsrc_t rd = mk_rsrc(wb + OFF_DYN, DYN_BYTES), rw = mk_rsrc(wb, (unsigned)OFF_DYN);
#pragma unroll
  for (int i = 0; i < 7; ++i) {
    const int f = i * 8 + wave;
    const unsigned src = f < 28 ? (unsigned)OFF_WKF + h * 28672u + f * 1024u : (unsigned)OFF_WVF + h * 28672u + (f - 28) * 1024u;
    lds_dma(rw, lds + f * 1024, lane * 16u, src);
  }
  const int s = part * 8 + wave;
  const u16* xp0 = in.Ax + (size_t)(s * 32 + rho) * DM + g * 8;
  const u16* xp1 = xp0 + 16 * DM;
  v4u X0[KS], X1[KS];
#pragma unroll
  for (int k = 0; k < KS; ++k) { X0[k] = *(const v4u*)(xp0 + k * 32); X1[k] = *(const v4u*)(xp1 + k * 32); }
  DRAIN_BARRIER();
  f32x4 pk_[2][2], vv[2][2];
#pragma unroll
  for (int t = 0; t < 2; ++t)
#pragma unroll
    for (int b = 0; b < 2; ++b) { pk_[t][b] = Z4; vv[t][b] = Z4; }
#pragma unroll
  for (int k = 0; k < KS; ++k) {
    const v4u k0 = *(const v4u*)(lds + k * 1024 + lane * 16), k1 = *(const v4u*)(lds + (KS + k) * 1024 + lane * 16);
    const v4u v0 = *(const v4u*)(lds + 28672 + k * 1024 + lane * 16), v1 = *(const v4u*)(lds + 28672 + (KS + k) * 1024 + lane * 16);
    pk_[0][0] = mfma32(X0[k], k0, pk_[0][0]); pk_[0][1] = mfma32(X0[k], k1, pk_[0][1]);
    pk_[1][0] = mfma32(X1[k], k0, pk_[1][0]); pk_[1][1] = mfma32(X1[k], k1, pk_[1][1]);
    vv[0][0] = mfma32(X0[k], v0, vv[0][0]); vv[0][1] = mfma32(X0[k], v1, vv[0][1]);
    vv[1][0] = mfma32(X1[k], v0, vv[1][0]); vv[1][1] = mfma32(X1[k], v1, vv[1][1]);
  }
  const bool pad = rho >= 12;
  f32x4 pp[2][2], pn[2][2];
#pragma unroll
  for (int t = 0; t < 2; ++t)
#pragma unroll
    for (int r = 0; r < 4; ++r) {
      const float a0 = pk_[t][0][r], a1 = pad ? a0 : pk_[t][1][r];
      const float mx = row16_max(vmax(a0, a1));
      const float mn = -row16_max(vmax(-a0, -a1));
      const float ep0 = __builtin_amdgcn_exp2f(a0 - mx), ep1 = pad ? 0.f : __builtin_amdgcn_exp2f(a1 - mx);
      const float en0 = __builtin_amdgcn_exp2f(mn - a0), en1 = pad ? 0.f : __builtin_amdgcn_exp2f(mn - a1);
      const float ip = __builtin_amdgcn_rcpf(row16_sum(ep0 + ep1)), in_ = __builtin_amdgcn_rcpf(row16_sum(en0 + en1));
      pp[t][0][r] = ep0 * ip; pp[t][1][r] = ep1 * ip; pn[t][0][r] = en0 * in_; pn[t][1][r] = en1 * in_;
    }
  if (rho == 12) { vv[0][1] = (f32x4){1.f, 1.f, 1.f, 1.f}; vv[1][1] = (f32x4){1.f, 1.f, 1.f, 1.f}; }
  const v4u vb0 = pack8(vv[0][0], vv[1][0]), vb1 = pack8(vv[0][1], vv[1][1]);
  v4u fa[4] = {pack8(pp[0][0], pp[1][0]), pack8(pp[0][1], pp[1][1]), pack8(pn[0][0], pn[1][0]), pack8(pn[0][1], pn[1][1])};
  BARRIER();
  float* red = (float*)lds;
#pragma unroll
  for (int a = 0; a < 4; ++a) {
    *(f32x4*)(red + ((wave * 8 + a * 2 + 0) * 64 + lane) * 4) = mfma32(fa[a], vb0, Z4);
    *(f32x4*)(red + ((wave * 8 + a * 2 + 1) * 64 + lane) * 4) = mfma32(fa[a], vb1, Z4);
  }
  BARRIER();
  {
    f32x4 sum = Z4;
#pragma unroll
    for (int w = 0; w < 8; ++w) sum += *(const f32x4*)(red + (w * 512 + tid) * 4);
    st_sc1(rd, DY_KVP + (unsigned)(part * NH + h) * 8192u + tid * 16u, __builtin_bit_cast(v4u, sum));
  }
  DRAIN_BARRIER();
  const int arrived = ticket(wb, CT_HCNT + h * 128, lds, tid);
  if (arrived == 7) {
    if (wave < 4) {
      const int sg = wave >> 1, b = wave & 1;
      v4u lo[8], hi[8];
#pragma unroll
      for (int p = 0; p < 8; ++p) {
        lo[p] = ld_sc1s(rd, lane * 16u, DY_KVP + (unsigned)(p * NH + h) * 8192u + (unsigned)((sg * 2 + 0) * 2 + b) * 1024u);
        hi[p] = ld_sc1s(rd, lane * 16u, DY_KVP + (unsigned)(p * NH + h) * 8192u + (unsigned)((sg * 2 + 1) * 2 + b) * 1024u);
      }
      f32x4 l4 = Z4, h4 = Z4;
#pragma unroll
      for (int p = 0; p < 8; ++p) { l4 += __builtin_bit_cast(f32x4, lo[p]); h4 += __builtin_bit_cast(f32x4, hi[p]); }
      st_sc1(rd, DY_KV + (unsigned)(h * 4 + wave) * 1024u + lane * 16u, pack8(l4, h4));
    }
    DRAIN_BARRIER();
    if (tid == 0) __hip_atomic_fetch_add((GAS unsigned*)(wb + OFF_CTRL + CT_ACNT), 1u, __ATOMIC_RELAXED, __HIP_MEMORY_SCOPE_AGENT);
  }
}

DEV void parta_unit2(const In& in, unsigned char* wb, int u, unsigned char* lds, int tid) {
  const int lane = tid & 63, wave = rfl(tid >> 6);
  const int rho = lane & 15, g = lane >> 4;
  const rsrc_t rd = mk_rsrc(wb + OFF_DYN, DYN_BYTES), rc = mk_rsrc(wb + OFF_CTRL, CTRL_BYTES);
  const rsrc_t rw = mk_rsrc(wb, (unsigned)OFF_DYN);
  int budget = SPIN_BUDGET;
#pragma unroll
  for (int i = 0; i < 4; ++i) {
    const int f = i * 8 + wave;
    if (f < 28) {
      const int tt = f / KS, ks = f % KS;
      __builtin_amdgcn_global_load_lds((const unsigned*)(in.Ax + (size_t)(u * 32 + tt * 16 + rho) * DM + ks * 32 + g * 8),
                                       (LAS unsigned*)(lds + f * 1024), 16, 0, 0);
    }
  }
  unsigned char* ob_l = lds + 28672;
  f32x4 p[2][2][2];
#pragma unroll
  for (int hh = 0; hh < 2; ++hh) {
    const unsigned wq = (unsigned)OFF_WQF + (unsigned)(2 * wave + hh) * 28672u;
    v4u Wf[2][KS];
    asm volatile("" ::: "memory");
#pragma unroll
    for (int k = 0; k < KS; ++k) { Wf[0][k] = ld_s(rw, lane * 16u, wq + k * 1024); Wf[1][k] = ld_s(rw, lane * 16u, wq + (KS + k) * 1024); }
    if (hh == 0) DRAIN_BARRIER();
    p[hh][0][0] = Z4; p[hh][0][1] = Z4; p[hh][1][0] = Z4; p[hh][1][1] = Z4;
#pragma unroll
    for (int k = 0; k < KS; ++k) {
      const v4u x0 = *(const v4u*)(lds + k * 1024 + lane * 16), x1 = *(const v4u*)(lds + (KS + k) * 1024 + lane * 16);
      p[hh][0][0] = mfma32(Wf[0][k], x0, p[hh][0][0]); p[hh][1][0] = mfma32(Wf[1][k], x0, p[hh][1][0]);
      p[hh][0][1] = mfma32(Wf[0][k], x1, p[hh][0][1]); p[hh][1][1] = mfma32(Wf[1][k], x1, p[hh][1][1]);
    }
    PIN2(p[hh][0][0], p[hh][0][1]); PIN2(p[hh][1][0], p[hh][1][1]);
  }
  wait_words(rc, lane == 0 ? (unsigned)CT_ACNT : OOB, NH, budget, true);
  const bool pad = g == 3;
#pragma unroll
  for (int hh = 0; hh < 2; ++hh) {
    const int h = 2 * wave + hh;
    v4u kvf[4];
#pragma unroll
    for (int a = 0; a < 4; ++a) kvf[a] = ld_sc1s(rd, lane * 16u, DY_KV + (unsigned)(h * 4 + a) * 1024u);
#pragma unroll
    for (int tt = 0; tt < 2; ++tt) {
      const f32x4 p0 = p[hh][0][tt], p1 = pad ? p0 : p[hh][1][tt];
      float mx = vmax(vmax(vmax(p0[0], p0[1]), vmax(p0[2], p0[3])), vmax(vmax(p1[0], p1[1]), vmax(p1[2], p1[3])));
      float mn = vmax(vmax(vmax(-p0[0], -p0[1]), vmax(-p0[2], -p0[3])), vmax(vmax(-p1[0], -p1[1]), vmax(-p1[2], -p1[3])));
      mx = xg_max(mx); mn = -xg_max(mn);
      f32x4 ep0, ep1, en0, en1;
      float sp = 0.f, sn = 0.f;
#pragma unroll
      for (int r = 0; r < 4; ++r) {
        ep0[r] = __builtin_amdgcn_exp2f(p0[r] - mx); ep1[r] = pad ? 0.f : __builtin_amdgcn_exp2f(p1[r] - mx);
        en0[r] = __builtin_amdgcn_exp2f(mn - p0[r]); en1[r] = pad ? 0.f : __builtin_amdgcn_exp2f(mn - p1[r]);
        sp += ep0[r] + ep1[r]; sn += en0[r] + en1[r];
      }
      const float ip = __builtin_amdgcn_rcpf(xg_sum(sp)), in_ = __builtin_amdgcn_rcpf(xg_sum(sn));
      const v4u bp = pack8(ep0 * ip, ep1 * ip), bn = pack8(en0 * in_, en1 * in_);
      f32x4 n0 = mfma32(kvf[0], bp, Z4), n1 = mfma32(kvf[1], bp, Z4);
      n0 = mfma32(kvf[2], bn, n0); n1 = mfma32(kvf[3], bn, n1);
      const float den = xg_sum(pad ? n1[0] : 0.f) + 1e-6f;
      const float id = 1.f / den;
      if (pad) n1 = Z4;
      *(v4u*)(ob_l + (h * 2 + tt) * 1024 + lane * 16) = pack8(n0 * id, n1 * id);
    }
  }
  const int wv = wave < 7 ? wave : 6;
  const unsigned wo = (unsigned)OFF_WOF + (unsigned)(wv * 4) * 1024u;
  f32x4 acc[4][2];
#pragma unroll
  for (int n = 0; n < 4; ++n) { acc[n][0] = Z4; acc[n][1] = Z4; }
#pragma unroll
  for (int hb = 0; hb < 2; ++hb) {
    v4u Wo_[8][4];
    asm volatile("" ::: "memory");
#pragma unroll
    for (int h8 = 0; h8 < 8; ++h8)
#pragma unroll
      for (int n = 0; n < 4; ++n) Wo_[h8][n] = ld_s(rw, lane * 16u, wo + (unsigned)((hb * 8 + h8) * CT + n) * 1024u);
    if (hb == 0) BARRIER();
#pragma unroll
    for (int h8 = 0; h8 < 8; ++h8) {
      const int h = hb * 8 + h8;
      const v4u o0 = *(const v4u*)(ob_l + (h * 2) * 1024 + lane * 16), o1 = *(const v4u*)(ob_l + (h * 2 + 1) * 1024 + lane * 16);
#pragma unroll
      for (int n = 0; n < 4; ++n) { acc[n][0] = mfma32(Wo_[h8][n], o0, acc[n][0]); acc[n][1] = mfma32(Wo_[h8][n], o1, acc[n][1]); }
    }
#pragma unroll
    for (int n = 0; n < 4; ++n) PIN2(acc[n][0], acc[n][1]);
  }
  if (wave < 7) {
#pragma unroll
    for (int tt = 0; tt < 2; ++tt)
#pragma unroll
      for (int np = 0; np < 2; ++np)
        *(v4u*)(in.out + (size_t)(u * 32 + tt * 16 + rho) * 896 + (wave * 2 + np) * 32 + 8 * g) = pack8(acc[2 * np][tt], acc[2 * np + 1][tt]);
  }
}

DEV void parta_all(const In& in, unsigned char* wb, unsigned char* lds, int tid) {
  for (;;) { const int u = ticket(wb, CT_Q1, lds, tid); if (u >= 128) break; parta_unit1(in, wb, u, lds, tid); }
  for (;;) { const int u = ticket(wb, CT_Q2, lds, tid); if (u >= 64) break; parta_unit2(in, wb, u, lds, tid); }
}

DEV void unit_XS(unsigned char* wb, int s, int hq, unsigned char* lds, int tid) {
  const int lane = tid & 63, wave = rfl(tid >> 6);
  const int rho = lane & 15, g = lane >> 4;
  const int h = hq * 4 + (wave >> 1), tt = wave & 1;
  const rsrc_t rw = mk_rsrc(wb, (unsigned)OFF_DYN);
  const rsrc_t rd = mk_rsrc(wb + OFF_DYN, DYN_BYTES), rc = mk_rsrc(wb + OFF_CTRL, CTRL_BYTES);
  int budget = SPIN_BUDGET;
  const int tok = sig(tt, rho);
  v4u X[KS];
#pragma unroll
  for (int k = 0; k < KS; ++k) {
    const v4u xv = ld_s(rw, lane * 16u, (unsigned)OFF_XFR + (unsigned)((s * 2 + tt) * KS + k) * 1024u);
    auto e0 = __builtin_amdgcn_permlane16_swap(xv.x, xv.y, false, false);
    auto e1 = __builtin_amdgcn_permlane16_swap(xv.z, xv.w, false, false);
    X[k] = (v4u){e0[0], e1[0], e0[1], e1[1]};
  }
  if (wave == 0) wait_words(rc, lane < 14 ? (unsigned)(CT_PROG + (hq * 14 + lane) * 128) : OOB, s + 1, budget, true);
  BARRIER();
  const unsigned gp = DY_GT + (unsigned)s * SEGB + (unsigned)(h * 2 * KS) * 1024u;
  f32x4 sx0 = Z4, sx1 = Z4, sm = Z4;
  {
    v4u G0[KS], G1[KS];
#pragma unroll
    for (int k = 0; k < KS; ++k) { G0[k] = ld_sc1s(rd, lane * 16u, gp + k * 1024); G1[k] = ld_sc1s(rd, lane * 16u, gp + (KS + k) * 1024); }
#pragma unroll
    for (int k = 0; k < KS; ++k) { sx0 = mfma32(G0[k], X[k], sx0); sx1 = mfma32(G1[k], X[k], sx1); }
    PIN2(sx0, sx1);
  }
  if (wave == 0) {
    wait_words(rc, lane == 0 ? (unsigned)CT_STEP : OOB, s < NSEG - 2 ? s + 2 : NSEG - 2, budget, true);
  }
  BARRIER();
  {
    const int key = ((rho & 7) >> 1) + 4 * (rho & 1);
    const unsigned rl = (unsigned)(g * 8 + key) * 16u;
    if (s >= NSEG - 2) poll1(rd, lane < KS ? DY_RC + (unsigned)((s * NH + h) * KS + lane) * 512u : OOB, budget);
    v4u R[KS];
    bool bad;
#pragma nounroll
    do {
      asm volatile("" ::: "memory");
      unsigned mm = 0;
#pragma unroll
      for (int k = 0; k < KS; ++k) R[k] = ld_sc1s(rd, rl, DY_RC + (unsigned)((s * NH + h) * KS + k) * 512u);
#pragma unroll
      for (int k = 0; k < KS; ++k) mm = max(mm, max(max(R[k].x, R[k].y), max(R[k].z, R[k].w)));
      bad = __any(mm == 0xFFFFFFFFu);
      budget -= 4;
    } while (bad && budget > 0);
#pragma unroll
    for (int k = 0; k < KS; ++k) sm = mfma32(R[k], X[k], sm);
  }
  float v[8], w[4];
  float m = -3.0e38f;
#pragma unroll
  for (int j = 0; j < 8; ++j) { v[j] = (8 * g + j <= tok) ? (j < 4 ? sx0[j & 3] : sx1[j & 3]) : -3.0e38f; m = vmax(m, v[j]); }
#pragma unroll
  for (int j = 0; j < 4; ++j) { w[j] = g < 2 ? sm[j] : -3.0e38f; m = vmax(m, w[j]); }
  m = xg_max(m);
  float l = 0.f;
#pragma unroll
  for (int j = 0; j < 8; ++j) { v[j] = (8 * g + j <= tok) ? __builtin_amdgcn_exp2f(v[j] - m) : 0.f; l += v[j]; }
#pragma unroll
  for (int j = 0; j < 4; ++j) { w[j] = g < 2 ? __builtin_amdgcn_exp2f(w[j] - m) : 0.f; l += w[j]; }
  l = xg_sum(l);
  const float il = __builtin_amdgcn_rcpf(l);
  const v4u px = {pk2(v[0] * il, v[1] * il), pk2(v[2] * il, v[3] * il), pk2(v[4] * il, v[5] * il), pk2(v[6] * il, v[7] * il)};
  const v2u pm = {min(pk2(w[0] * il, w[1] * il), 0xFFFFFFFEu), min(pk2(w[2] * il, w[3] * il), 0xFFFFFFFEu)};
  st_sc1(rd, DY_PX + (unsigned)((s * NH + h) * 2 + tt) * 1024u + lane * 16u, nosentu(px));
  __builtin_amdgcn_raw_buffer_store_b64(pm, rd, g < 2 ? DY_PM + (unsigned)((s * 4 + hq) * 2 + tt) * 1024u + (unsigned)((h & 3) * 16 + rho) * 16u + 8u * g : OOB, 0, 16);
  DRAIN_BARRIER();
  if (tid == 0) __hip_atomic_fetch_add((GAS unsigned*)(wb + OFF_CTRL + CT_XCNT + s * 128), 1u, __ATOMIC_RELAXED, __HIP_MEMORY_SCOPE_AGENT);
}

DEV void unit_XO(const In& in, unsigned char* wb, int s, int qt, unsigned char* lds, int tid) {
  const int lane = tid & 63, wave = rfl(tid >> 6);
  const int rho = lane & 15, g = lane >> 4;
  const rsrc_t rd = mk_rsrc(wb + OFF_DYN, DYN_BYTES), rc = mk_rsrc(wb + OFF_CTRL, CTRL_BYTES);
  int budget = SPIN_BUDGET;
  const int ct = qt * 7 + (wave < 7 ? wave : 6);
  if (wave == 0) wait_words(rc, lane < 56 ? (unsigned)(CT_PROG + (56 + lane) * 128) : OOB, s + 1, budget, true);
  BARRIER();
  v4u U[NH];
#pragma unroll
  for (int h = 0; h < NH; ++h) U[h] = ld_sc1s(rd, lane * 16u, DY_UT + (unsigned)s * SEGB + (unsigned)(h * CT + ct) * 1024u);
  if (wave == 0) {
    wait_words(rc, lane == 0 ? (unsigned)CT_STEP : OOB, s < NSEG - 2 ? s + 2 : NSEG - 2, budget, true);
  }
  BARRIER();
  v4u V[4];
  {
    bool bad;
#pragma nounroll
    do {
      asm volatile("" ::: "memory");
      unsigned mm = 0;
#pragma unroll
      for (int hq = 0; hq < 4; ++hq) V[hq] = ld_sc1s(rd, lane * 16u, DY_VC + (unsigned)((s * 4 + hq) * CT + ct) * 1024u);
#pragma unroll
      for (int hq = 0; hq < 4; ++hq) mm = max(mm, max(max(V[hq].x, V[hq].y), max(V[hq].z, V[hq].w)));
      bad = __any(mm == 0xFFFFFFFFu);
      budget -= 2;
    } while (bad && budget > 0);
  }
  if (wave == 0 && s < NSEG - 2) wait_words(rc, lane == 0 ? (unsigned)(CT_XCNT + s * 128) : OOB, 4, budget, true);
  BARRIER();
  {
    if (s >= NSEG - 2) poll1(rd, lane < 32 ? DY_PX + (unsigned)s * 32768u + lane * 1024u : OOB, budget);
    v4u t[5];
    bool bad;
#pragma nounroll
    do {
      asm volatile("" ::: "memory");
      unsigned mm = 0;
#pragma unroll
      for (int i = 0; i < 4; ++i) t[i] = ld_sc1s(rd, tid * 16u, DY_PX + (unsigned)s * 32768u + i * 8192u);
      t[4] = ld_sc1s(rd, tid * 16u, DY_PM + (unsigned)s * 8192u);
#pragma unroll
      for (int i = 0; i < 5; ++i) mm = max(mm, max(max(t[i].x, t[i].y), max(t[i].z, t[i].w)));
      bad = __any(mm == 0xFFFFFFFFu);
      budget -= 2;
    } while (bad && budget > 0);
#pragma unroll
    for (int i = 0; i < 5; ++i) *(v4u*)(lds + (i * 512 + tid) * 16) = t[i];
  }
  BARRIER();
  f32x4 a0 = Z4, a1 = Z4;
#pragma unroll
  for (int h = 0; h < NH; ++h) {
    a0 = mfma32(U[h], *(const v4u*)(lds + (h * 2) * 1024 + lane * 16), a0);
    a1 = mfma32(U[h], *(const v4u*)(lds + (h * 2 + 1) * 1024 + lane * 16), a1);
  }
#pragma unroll
  for (int hq = 0; hq < 4; ++hq) {
    a0 = mfma32(V[hq], *(const v4u*)(lds + 32768 + (hq * 2) * 1024 + lane * 16), a0);
    a1 = mfma32(V[hq], *(const v4u*)(lds + 32768 + (hq * 2 + 1) * 1024 + lane * 16), a1);
  }
  if (wave < 7) {
    v2u o0 = {pk2(a0[0], a0[1]), pk2(a0[2], a0[3])}, o1 = {pk2(a1[0], a1[1]), pk2(a1[2], a1[3])};
    *(v2u*)(in.out + (size_t)(s * 32 + sig(0, rho)) * 896 + 448 + ct * 16 + 4 * g) = o0;
    *(v2u*)(in.out + (size_t)(s * 32 + sig(1, rho)) * 896 + 448 + ct * 16 + 4 * g) = o1;
  }
}

DEV void role_fill(unsigned char* wb, int j, int tid) {
  const unsigned par = read_parity(wb, tid & 63);
  const rsrc_t rx = mk_rsrc(wb + OFF_XCH, (unsigned)XCH_BYTES);
  const v4u ones = {~0u, ~0u, ~0u, ~0u};
  for (int p = j; p < XCH_PAIRS_USED; p += 8) {
    const bool done = p <= (int)FSTEPS || (p >= 64 && p < 64 + 4 * (int)FSTEPS);
    if (!done) st_sc1(rx, pair_off(par, p) + tid * 16u, ones);
  }
  DRAIN_BARRIER();
  if (tid == 0) __hip_atomic_fetch_add((GAS unsigned*)(wb + OFF_CTRL + CT_FILL), 1u, __ATOMIC_RELAXED, __HIP_MEMORY_SCOPE_AGENT);
}

DEV int get_slot(unsigned char* wb, unsigned char* lds, int tid) {
  int* sl = (int*)lds;
  if (tid == 0) {
    GAS unsigned* tick = (GAS unsigned*)(wb + OFF_CTRL + CT_TICK);
    const unsigned x = xcc_id() & 7u;
    const unsigned r = __hip_atomic_fetch_add(tick + x * 64, 1u, __ATOMIC_RELAXED, __HIP_MEMORY_SCOPE_AGENT);
    int slot;
    if (r < 32) slot = (int)(x * 32 + r);
    else {
      unsigned c[8]; int spins = 0;
      for (;;) {
        unsigned tot = 0;
#pragma unroll
        for (int k = 0; k < 8; ++k) { c[k] = __hip_atomic_load(tick + k * 64, __ATOMIC_RELAXED, __HIP_MEMORY_SCOPE_AGENT); tot += c[k]; }
        if (tot >= 256u || ++spins > SPIN_BUDGET) break;
      }
      unsigned idx = r - 32;
#pragma unroll
      for (int k = 0; k < 8; ++k) idx += ((unsigned)k < x && c[k] > 32) ? c[k] - 32 : 0;
      slot = -1;
#pragma unroll
      for (int k = 0; k < 8; ++k) {
        const unsigned fr = c[k] < 32 ? 32 - c[k] : 0;
        if (slot < 0) { if (idx < fr) slot = (int)(k * 32 + c[k] + idx); else idx -= fr; }
      }
    }
    sl[0] = slot;
  }
  __syncthreads();
  const int slot = rfl(sl[0]);
  __syncthreads();
  return slot;
}

constexpr int MAIN_LDS = 114688;
__global__ void __launch_bounds__(512, 2) k_main(In in) {
  extern __shared__ __attribute__((aligned(16))) unsigned char lds[];
  unsigned char* wb = ws_base(in.ws);
  const int tid = threadIdx.x;
  const int slot = get_slot(wb, lds, tid);
  if (slot < 0) return;
  const int idx = (slot & 31) * 4 + ((slot >> 5) & 3);
  if (slot < 128) {
    if (idx < 64) {
      const int h = idx >> 2, q = idx & 3;
      if (q < 2) role_S<true>(wb, h, q, lds, tid); else role_S<false>(wb, h, q, lds, tid);
    } else if (idx < 120) {
      const int o = idx - 64;
      role_O(wb, (o >> 2) & 1, (o >> 3) * 4 + (o & 3), lds, tid);
    } else role_fill(wb, idx - 120, tid);
  } else {
    if (idx < 56) gu_worker<true>(wb, idx, lds, tid);
    else if (idx < 112) gu_worker<false>(wb, idx, lds, tid);
    else for (int i = 0; i < MDELAY; ++i) __builtin_amdgcn_s_sleep(16);
    parta_all(in, wb, lds, tid);
    for (;;) {
      const int u = ticket(wb, CT_Q3, lds, tid);
      if (u >= NSEG * 8) break;
      if (u & 4) unit_XO(in, wb, u >> 3, u & 3, lds, tid); else unit_XS(wb, u >> 3, u & 3, lds, tid);
      if (u >= NSEG * 8 - 16) break;
    }
  }
}

extern "C" void kernel_launch(void* const* d_in, const int* in_sizes, int n_in,
                              void* d_out, int out_size, void* d_ws, size_t ws_size,
                              hipStream_t stream) {
  In in;
  in.Ax = (const u16*)d_in[0]; in.Awqkv = (const u16*)d_in[1]; in.Awphi = (const u16*)d_in[2]; in.Awo = (const u16*)d_in[3];
  in.Bx = (const u16*)d_in[4]; in.Bmem0 = (const u16*)d_in[5]; in.Wq = (const u16*)d_in[6]; in.Wk = (const u16*)d_in[7];
  in.Wv = (const u16*)d_in[8]; in.Wo = (const u16*)d_in[9];
  in.out = (u16*)d_out; in.ws = d_ws;
  (void)hipFuncSetAttribute((const void*)k_prep, hipFuncAttributeMaxDynamicSharedMemorySize, 57344);
  (void)hipFuncSetAttribute((const void*)k_main, hipFuncAttributeMaxDynamicSharedMemorySize, MAIN_LDS);
  k_prep<<<PREP_GRID, 448, 57344, stream>>>(in);
  k_main<<<256, 512, MAIN_LDS, stream>>>(in);
}
```
